```python
import torch

def _softmax(x, axis=-1):
    return torch.softmax(x, dim=axis)
def _gelu(x):
    return torch.nn.functional.gelu(x, approximate="tanh")
def _silu(x):
    return torch.nn.functional.silu(x)
def _sigmoid(x):
    return torch.sigmoid(x)
def _layernorm(x, axis=-1, eps=1e-5):
    m = x.mean(dim=axis, keepdim=True)
    v = x.var(dim=axis, keepdim=True, unbiased=False)
    return (x - m) / torch.sqrt(v + eps)
def _scatter_add(tgt, idx, src, axis=0):
    return tgt.clone().index_add_(axis, idx, src)
def _segment_sum(src, seg, n):
    out = torch.zeros((n,) + src.shape[1:], dtype=src.dtype)
    return out.index_add_(0, seg, src)
def _pad(x, pw):
    flat = tuple(v for pair in reversed(pw) for v in pair)
    return torch.nn.functional.pad(x, flat)

kernel_name = 'sequential_preLN__sliding_block_cached__glu_gated_swiglu__decode'

def setup_inputs():

    _w = (lambda a: (lambda t: t.to(torch.bfloat16) if t.is_floating_point() else t)(torch.as_tensor(a)))
    def _A_setup():
        B, Sq, D = 1, 1, 512
        Hq, Hkv, Sk, Dh = 32, 8, 65536, 128
        assert Hq % Hkv == 0, (Hq, Hkv)
        return {
            "x": _w((lambda *s: torch.randn(*s, dtype=torch.bfloat16) * 0.05)(B, Sq, D)),
            "k_cache": _w((lambda *s: torch.randn(*s, dtype=torch.bfloat16) * 1.6)(B, Hkv, Sk, Dh)),
            "v_cache": _w((lambda *s: torch.randn(*s, dtype=torch.bfloat16) * 0.8)(B, Hkv, Sk, Dh)),
            "wq": _w((lambda *s: torch.randn(*s, dtype=torch.bfloat16) * 1.6)(D, Hq * Dh) * (D ** -0.5)),
            "wo": _w((lambda *s: torch.randn(*s, dtype=torch.bfloat16) * 0.8)(Hq * Dh, D) * ((Hq * Dh) ** -0.5)),
        }
    _A_in = _A_setup()

    def _B_setup():
        B, S, D, F = 1, 1, 512, 1792
        return {
            "x": _w((lambda *s: torch.randn(*s, dtype=torch.bfloat16) * 0.05)(B, S, D)),
            "w1": _w((lambda *s: torch.randn(*s, dtype=torch.bfloat16) * 0.4)(D, F) * (2.0 / D) ** 0.5),
            "w3": _w((lambda *s: torch.randn(*s, dtype=torch.bfloat16) * 0.4)(D, F) * (2.0 / D) ** 0.5),
            "w2": _w((lambda *s: torch.randn(*s, dtype=torch.bfloat16) * 0.4)(F, D) * (2.0 / F) ** 0.5),
        }
    _B_in = _B_setup()

    return {
        "A_x": _A_in["x"],
        "A_k_cache": _A_in["k_cache"],
        "A_v_cache": _A_in["v_cache"],
        "A_wq": _A_in["wq"],
        "A_wo": _A_in["wo"],
        "B_x": _B_in["x"],
        "B_w1": _B_in["w1"],
        "B_w3": _B_in["w3"],
        "B_w2": _B_in["w2"],
    }

def reference(A_x, A_k_cache, A_v_cache, A_wq, A_wo, B_x, B_w1, B_w3, B_w2):
    def _A_ref(x, k_cache, v_cache, wq, wo):
        B, Sq, D = x.shape
        Hq, Dh = 32, 128
        Hkv, Sk = k_cache.shape[1], k_cache.shape[2]
        W = min(4096, max(256, Sk // 4))
        rep = Hq // Hkv
        q = (lambda x, *d: x.permute(*d))((x @ wq).reshape(B, Sq, Hq, Dh), 0, 2, 1, 3)
        k = (lambda x, n, axis: torch.repeat_interleave(x, n, dim=axis))(k_cache[:, :, Sk - W:, :], rep, axis=1)
        v = (lambda x, n, axis: torch.repeat_interleave(x, n, dim=axis))(v_cache[:, :, Sk - W:, :], rep, axis=1)
        scores = torch.einsum("bhqd,bhkd->bhqk", q, k) * (Dh ** -0.5)
        p = _softmax(scores, axis=-1)
        o = torch.einsum("bhqk,bhkd->bhqd", p, v)
        return (lambda x, *d: x.permute(*d))(o, 0, 2, 1, 3).reshape(B, Sq, Hq * Dh) @ wo

    def _B_ref(x, w1, w3, w2):
        gate = torch.einsum("bsd,df->bsf", x, w1)
        up = torch.einsum("bsd,df->bsf", x, w3)
        h = _silu(gate) * up
        out = torch.einsum("bsf,fd->bsd", h, w2)
        return out

    x = A_x
    h = x + _A_ref(_layernorm(x, axis=-1), A_k_cache, A_v_cache, A_wq, A_wo)
    h2 = h + _B_ref(_layernorm(h, axis=-1), B_w1, B_w3, B_w2)
    return h2
```

```cpp
#include <hip/hip_runtime.h>
#include <hip/hip_bf16.h>
#include <stdint.h>

typedef unsigned short bf16_t;
typedef unsigned v4u __attribute__((ext_vector_type(4)));
__device__ __forceinline__ float bf2f(unsigned short b) { return __uint_as_float(((unsigned)b) << 16); }
__device__ __forceinline__ float bflo(unsigned u) { return __uint_as_float(u << 16); }
__device__ __forceinline__ float bfhi(unsigned u) { return __uint_as_float(u & 0xffff0000u); }
__device__ __forceinline__ float rbf(float f) { return __bfloat162float(__float2bfloat16(f)); }

constexpr int D = 512, HQ = 32, HKV = 8, SK = 65536, DH = 128, W = 4096, F = 1792;

__device__ void ln512(const float* in, float* out, float* red) {
  int t = threadIdx.x;
  float a = in[t], b = in[t + 256];
  float s = a + b;
  for (int o = 32; o; o >>= 1) s += __shfl_xor(s, o);
  if ((t & 63) == 0) red[t >> 6] = s;
  __syncthreads();
  float mean = (red[0] + red[1] + red[2] + red[3]) * (1.f / 512);
  __syncthreads();
  float da = a - mean, db = b - mean;
  float v = da * da + db * db;
  for (int o = 32; o; o >>= 1) v += __shfl_xor(v, o);
  if ((t & 63) == 0) red[t >> 6] = v;
  __syncthreads();
  float var = (red[0] + red[1] + red[2] + red[3]) * (1.f / 512);
  float r = 1.f / sqrtf(var + 1e-5f);
  out[t] = rbf(da * r); out[t + 256] = rbf(db * r);
  __syncthreads();
}

template <int K>
__device__ void gemv64(const float* xs, const bf16_t* Wm, int ldw, int n0, float* part  , float* y) {
  int t = threadIdx.x, r = t >> 3, c = t & 7;
  float acc[8] = {0, 0, 0, 0, 0, 0, 0, 0};
#pragma unroll 8
  for (int k = r; k < K; k += 32) {
    v4u w = *(const v4u*)(Wm + (size_t)k * ldw + n0 + c * 8);
    float xv = xs[k];
    acc[0] += xv * bflo(w.x); acc[1] += xv * bfhi(w.x); acc[2] += xv * bflo(w.y); acc[3] += xv * bfhi(w.y);
    acc[4] += xv * bflo(w.z); acc[5] += xv * bfhi(w.z); acc[6] += xv * bflo(w.w); acc[7] += xv * bfhi(w.w);
  }
  for (int j = 0; j < 8; ++j) part[r * 64 + c * 8 + j] = acc[j];
  __syncthreads();
  if (t < 64) { float s = 0; for (int i = 0; i < 32; ++i) s += part[i * 64 + t]; y[t] = s; }
  __syncthreads();
}

__global__ void __launch_bounds__(256) k_q(const bf16_t* x, const bf16_t* wq, float* q) {
  __shared__ float xs[512], xn[512], red[4], part[2048], y[64];
  int t = threadIdx.x;
  xs[t] = bf2f(x[t]); xs[t + 256] = bf2f(x[t + 256]);
  __syncthreads();
  ln512(xs, xn, red);
  gemv64<512>(xn, wq, HQ * DH, blockIdx.x * 64, part, y);
  if (t < 64) q[blockIdx.x * 64 + t] = rbf(y[t]);
}

__global__ void __launch_bounds__(256) k_attn(const float* q, const bf16_t* kc, const bf16_t* vc, float* po, float* pml) {
  __shared__ float qs[512], sc[4][128], part[16][512], ml[8];
  int g = blockIdx.x & 7, s = blockIdx.x >> 3, t = threadIdx.x;
  qs[t] = q[g * 512 + t]; qs[t + 256] = q[g * 512 + t + 256];
  __syncthreads();
  const bf16_t* kb = kc + ((size_t)g * SK + (SK - W) + s * 128) * DH;
  const bf16_t* vb = vc + ((size_t)g * SK + (SK - W) + s * 128) * DH;
  int r = t >> 4, c = t & 15;
  for (int i = 0; i < 8; ++i) {
    int key = r + 16 * i;
    v4u w = *(const v4u*)(kb + key * DH + c * 8);
    float kf[8] = {bflo(w.x), bfhi(w.x), bflo(w.y), bfhi(w.y), bflo(w.z), bfhi(w.z), bflo(w.w), bfhi(w.w)};
    for (int h = 0; h < 4; ++h) {
      float d = 0;
      for (int j = 0; j < 8; ++j) d += kf[j] * qs[h * 128 + c * 8 + j];
      for (int o = 8; o; o >>= 1) d += __shfl_xor(d, o);
      if (c == 0) sc[h][key] = d * 0.08838834764831845f;
    }
  }
  __syncthreads();
  {
    int h = t >> 6, l = t & 63;
    float a = sc[h][l], b = sc[h][l + 64];
    float m = fmaxf(a, b);
    for (int o = 32; o; o >>= 1) m = fmaxf(m, __shfl_xor(m, o));
    a = __expf(a - m); b = __expf(b - m);
    float su = a + b;
    for (int o = 32; o; o >>= 1) su += __shfl_xor(su, o);
    sc[h][l] = a; sc[h][l + 64] = b;
    if (l == 0) { pml[((g * 4 + h) * 32 + s) * 2] = m; pml[((g * 4 + h) * 32 + s) * 2 + 1] = su; }
  }
  __syncthreads();
  float acc[4][8] = {};
  for (int i = 0; i < 8; ++i) {
    int key = r + 16 * i;
    v4u w = *(const v4u*)(vb + key * DH + c * 8);
    float vf[8] = {bflo(w.x), bfhi(w.x), bflo(w.y), bfhi(w.y), bflo(w.z), bfhi(w.z), bflo(w.w), bfhi(w.w)};
    for (int h = 0; h < 4; ++h) { float p = sc[h][key]; for (int j = 0; j < 8; ++j) acc[h][j] += p * vf[j]; }
  }
  for (int h = 0; h < 4; ++h) for (int j = 0; j < 8; ++j) part[r][h * 128 + c * 8 + j] = acc[h][j];
  __syncthreads();
  for (int e = t; e < 512; e += 256) {
    float su = 0; for (int i = 0; i < 16; ++i) su += part[i][e];
    int h = e >> 7, d = e & 127;
    po[((size_t)(g * 4 + h) * 32 + s) * 128 + d] = su;
  }
}

__global__ void __launch_bounds__(256) k_wo(const float* po, const float* pml, const bf16_t* wo, float* hp) {
  __shared__ float os[128], part[2048], y[64];
  int cb = blockIdx.x & 7, h = blockIdx.x >> 3, t = threadIdx.x;
  if (t < 128) {
    float M = -INFINITY;
    for (int s = 0; s < 32; ++s) M = fmaxf(M, pml[(h * 32 + s) * 2]);
    float L = 0, o = 0;
    for (int s = 0; s < 32; ++s) { float w = __expf(pml[(h * 32 + s) * 2] - M); L += w * pml[(h * 32 + s) * 2 + 1]; o += w * po[((size_t)h * 32 + s) * 128 + t]; }
    os[t] = rbf(o / L);
  }
  __syncthreads();
  gemv64<128>(os, wo + (size_t)h * 128 * D, D, cb * 64, part, y);
  if (t < 64) hp[h * 512 + cb * 64 + t] = y[t];
}

__global__ void __launch_bounds__(256) k_up(const bf16_t* x, const float* hp, const bf16_t* w1, const bf16_t* w3, float* hbuf, float* hm) {
  __shared__ float hs[512], hn[512], red[4], part[2048], y1[64], y3[64];
  int t = threadIdx.x;
  for (int e = t; e < 512; e += 256) { float s = 0; for (int h = 0; h < 32; ++h) s += hp[h * 512 + e]; hs[e] = rbf(bf2f(x[e]) + rbf(s)); if (blockIdx.x == 0) hbuf[e] = hs[e]; }
  __syncthreads();
  ln512(hs, hn, red);
  gemv64<512>(hn, w1, F, blockIdx.x * 64, part, y1);
  gemv64<512>(hn, w3, F, blockIdx.x * 64, part, y3);
  if (t < 64) { float g = rbf(y1[t]), u = rbf(y3[t]); float si = rbf(g / (1.f + __expf(-g))); hm[blockIdx.x * 64 + t] = rbf(si * u); }
}

__global__ void __launch_bounds__(256) k_down(const float* hbuf, const float* hm, const bf16_t* w2, __hip_bfloat16* out) {
  __shared__ float hs[F], part[2048], y[64];
  int t = threadIdx.x;
  for (int e = t; e < F; e += 256) hs[e] = hm[e];
  __syncthreads();
  gemv64<F>(hs, w2, D, blockIdx.x * 64, part, y);
  if (t < 64) out[blockIdx.x * 64 + t] = __float2bfloat16(hbuf[blockIdx.x * 64 + t] + rbf(y[t]));
}

extern "C" void kernel_launch(void* const* d_in, const int* in_sizes, int n_in, void* d_out, int out_size, void* d_ws, size_t ws_size, hipStream_t stream) {
  const bf16_t* x = (const bf16_t*)d_in[0];
  const bf16_t* kc = (const bf16_t*)d_in[1];
  const bf16_t* vc = (const bf16_t*)d_in[2];
  const bf16_t* wq = (const bf16_t*)d_in[3];
  const bf16_t* wo = (const bf16_t*)d_in[4];
  const bf16_t* w1 = (const bf16_t*)d_in[6];
  const bf16_t* w3 = (const bf16_t*)d_in[7];
  const bf16_t* w2 = (const bf16_t*)d_in[8];
  float* ws = (float*)d_ws;
  float* q = ws; float* po = ws + 4096; float* pml = po + 32 * 32 * 128; float* hp = pml + 2048; float* hbuf = hp + 32 * 512; float* hm = hbuf + 512;
  k_q<<<64, 256, 0, stream>>>(x, wq, q);
  k_attn<<<256, 256, 0, stream>>>(q, kc, vc, po, pml);
  k_wo<<<256, 256, 0, stream>>>(po, pml, wo, hp);
  k_up<<<28, 256, 0, stream>>>(x, hp, w1, w3, hbuf, hm);
  k_down<<<8, 256, 0, stream>>>(hbuf, hm, w2, (__hip_bfloat16*)d_out);
}
```

```cpp
#include <hip/hip_runtime.h>
#include <hip/hip_bf16.h>
#include <stdint.h>

typedef unsigned short bf16_t;
typedef unsigned v4u __attribute__((ext_vector_type(4)));
__device__ __forceinline__ float bf2f(unsigned short b) { return __uint_as_float(((unsigned)b) << 16); }
__device__ __forceinline__ float bflo(unsigned u) { return __uint_as_float(u << 16); }
__device__ __forceinline__ float bfhi(unsigned u) { return __uint_as_float(u & 0xffff0000u); }
__device__ __forceinline__ float rbf(float f) { return __bfloat162float(__float2bfloat16(f)); }

constexpr int D = 512, HQ = 32, HKV = 8, SK = 65536, DH = 128, W = 4096, F = 1792;

__device__ void ln512(const float* in, float* out, float* red) {
  int t = threadIdx.x;
  float a = in[t], b = in[t + 256];
  float s = a + b;
  for (int o = 32; o; o >>= 1) s += __shfl_xor(s, o);
  if ((t & 63) == 0) red[t >> 6] = s;
  __syncthreads();
  float mean = (red[0] + red[1] + red[2] + red[3]) * (1.f / 512);
  __syncthreads();
  float da = a - mean, db = b - mean;
  float v = da * da + db * db;
  for (int o = 32; o; o >>= 1) v += __shfl_xor(v, o);
  if ((t & 63) == 0) red[t >> 6] = v;
  __syncthreads();
  float var = (red[0] + red[1] + red[2] + red[3]) * (1.f / 512);
  float mb = rbf(mean);
  float den = rbf(sqrtf(rbf(rbf(var) + 1e-5f)));
  out[t] = rbf(rbf(a - mb) / den); out[t + 256] = rbf(rbf(b - mb) / den);
  __syncthreads();
}

template <int K>
__device__ void gemv64(const float* xs, const bf16_t* Wm, int ldw, int n0, float* part  , float* y) {
  int t = threadIdx.x, r = t >> 3, c = t & 7;
  float acc[8] = {0, 0, 0, 0, 0, 0, 0, 0};
#pragma unroll 8
  for (int k = r; k < K; k += 32) {
    v4u w = *(const v4u*)(Wm + (size_t)k * ldw + n0 + c * 8);
    float xv = xs[k];
    acc[0] += xv * bflo(w.x); acc[1] += xv * bfhi(w.x); acc[2] += xv * bflo(w.y); acc[3] += xv * bfhi(w.y);
    acc[4] += xv * bflo(w.z); acc[5] += xv * bfhi(w.z); acc[6] += xv * bflo(w.w); acc[7] += xv * bfhi(w.w);
  }
  for (int j = 0; j < 8; ++j) part[r * 64 + c * 8 + j] = acc[j];
  __syncthreads();
  if (t < 64) { float s = 0; for (int i = 0; i < 32; ++i) s += part[i * 64 + t]; y[t] = s; }
  __syncthreads();
}

__global__ void __launch_bounds__(256) k_q(const bf16_t* x, const bf16_t* wq, float* q) {
  __shared__ float xs[512], xn[512], red[4], part[2048], y[64];
  int t = threadIdx.x;
  xs[t] = bf2f(x[t]); xs[t + 256] = bf2f(x[t + 256]);
  __syncthreads();
  ln512(xs, xn, red);
  gemv64<512>(xn, wq, HQ * DH, blockIdx.x * 64, part, y);
  if (t < 64) q[blockIdx.x * 64 + t] = rbf(y[t]);
}

__global__ void __launch_bounds__(256) k_attn(const float* q, const bf16_t* kc, const bf16_t* vc, float* po, float* pml) {
  __shared__ float qs[512], sc[4][128], part[16][512], ml[8];
  int g = blockIdx.x & 7, s = blockIdx.x >> 3, t = threadIdx.x;
  qs[t] = q[g * 512 + t]; qs[t + 256] = q[g * 512 + t + 256];
  __syncthreads();
  const bf16_t* kb = kc + ((size_t)g * SK + (SK - W) + s * 128) * DH;
  const bf16_t* vb = vc + ((size_t)g * SK + (SK - W) + s * 128) * DH;
  int r = t >> 4, c = t & 15;
  for (int i = 0; i < 8; ++i) {
    int key = r + 16 * i;
    v4u w = *(const v4u*)(kb + key * DH + c * 8);
    float kf[8] = {bflo(w.x), bfhi(w.x), bflo(w.y), bfhi(w.y), bflo(w.z), bfhi(w.z), bflo(w.w), bfhi(w.w)};
    for (int h = 0; h < 4; ++h) {
      float d = 0;
      for (int j = 0; j < 8; ++j) d += kf[j] * qs[h * 128 + c * 8 + j];
      for (int o = 8; o; o >>= 1) d += __shfl_xor(d, o);
      if (c == 0) sc[h][key] = rbf(rbf(d) * 0.08838834764831845f);
    }
  }
  __syncthreads();
  {
    int h = t >> 6, l = t & 63;
    float a = sc[h][l], b = sc[h][l + 64];
    float m = fmaxf(a, b);
    for (int o = 32; o; o >>= 1) m = fmaxf(m, __shfl_xor(m, o));
    a = __expf(a - m); b = __expf(b - m);
    float su = a + b;
    for (int o = 32; o; o >>= 1) su += __shfl_xor(su, o);
    sc[h][l] = a; sc[h][l + 64] = b;
    if (l == 0) { pml[((g * 4 + h) * 32 + s) * 2] = m; pml[((g * 4 + h) * 32 + s) * 2 + 1] = su; }
  }
  __syncthreads();
  float acc[4][8] = {};
  for (int i = 0; i < 8; ++i) {
    int key = r + 16 * i;
    v4u w = *(const v4u*)(vb + key * DH + c * 8);
    float vf[8] = {bflo(w.x), bfhi(w.x), bflo(w.y), bfhi(w.y), bflo(w.z), bfhi(w.z), bflo(w.w), bfhi(w.w)};
    for (int h = 0; h < 4; ++h) { float p = sc[h][key]; for (int j = 0; j < 8; ++j) acc[h][j] += p * vf[j]; }
  }
  for (int h = 0; h < 4; ++h) for (int j = 0; j < 8; ++j) part[r][h * 128 + c * 8 + j] = acc[h][j];
  __syncthreads();
  for (int e = t; e < 512; e += 256) {
    float su = 0; for (int i = 0; i < 16; ++i) su += part[i][e];
    int h = e >> 7, d = e & 127;
    po[((size_t)(g * 4 + h) * 32 + s) * 128 + d] = su;
  }
}

__global__ void __launch_bounds__(256) k_wo(const float* po, const float* pml, const bf16_t* wo, float* hp) {
  __shared__ float os[128], part[2048], y[64];
  int cb = blockIdx.x & 7, h = blockIdx.x >> 3, t = threadIdx.x;
  if (t < 128) {
    float M = -INFINITY;
    for (int s = 0; s < 32; ++s) M = fmaxf(M, pml[(h * 32 + s) * 2]);
    float L = 0, o = 0;
    for (int s = 0; s < 32; ++s) { float w = __expf(pml[(h * 32 + s) * 2] - M); L += w * pml[(h * 32 + s) * 2 + 1]; o += w * po[((size_t)h * 32 + s) * 128 + t]; }
    os[t] = rbf(o / L);
  }
  __syncthreads();
  gemv64<128>(os, wo + (size_t)h * 128 * D, D, cb * 64, part, y);
  if (t < 64) hp[h * 512 + cb * 64 + t] = y[t];
}

__global__ void __launch_bounds__(256) k_up(const bf16_t* x, const float* hp, const bf16_t* w1, const bf16_t* w3, float* hbuf, float* hm) {
  __shared__ float hs[512], hn[512], red[4], part[2048], y1[64], y3[64];
  int t = threadIdx.x;
  for (int e = t; e < 512; e += 256) { float s = 0; for (int h = 0; h < 32; ++h) s += hp[h * 512 + e]; hs[e] = rbf(bf2f(x[e]) + rbf(s)); if (blockIdx.x == 0) hbuf[e] = hs[e]; }
  __syncthreads();
  ln512(hs, hn, red);
  gemv64<512>(hn, w1, F, blockIdx.x * 64, part, y1);
  gemv64<512>(hn, w3, F, blockIdx.x * 64, part, y3);
  if (t < 64) { float g = rbf(y1[t]), u = rbf(y3[t]); float si = rbf(g / (1.f + __expf(-g))); hm[blockIdx.x * 64 + t] = rbf(si * u); }
}

__global__ void __launch_bounds__(256) k_down(const float* hbuf, const float* hm, const bf16_t* w2, __hip_bfloat16* out) {
  __shared__ float hs[F], part[2048], y[64];
  int t = threadIdx.x;
  for (int e = t; e < F; e += 256) hs[e] = hm[e];
  __syncthreads();
  gemv64<F>(hs, w2, D, blockIdx.x * 64, part, y);
  if (t < 64) out[blockIdx.x * 64 + t] = __float2bfloat16(hbuf[blockIdx.x * 64 + t] + rbf(y[t]));
}

extern "C" void kernel_launch(void* const* d_in, const int* in_sizes, int n_in, void* d_out, int out_size, void* d_ws, size_t ws_size, hipStream_t stream) {
  const bf16_t* x = (const bf16_t*)d_in[0];
  const bf16_t* kc = (const bf16_t*)d_in[1];
  const bf16_t* vc = (const bf16_t*)d_in[2];
  const bf16_t* wq = (const bf16_t*)d_in[3];
  const bf16_t* wo = (const bf16_t*)d_in[4];
  const bf16_t* w1 = (const bf16_t*)d_in[6];
  const bf16_t* w3 = (const bf16_t*)d_in[7];
  const bf16_t* w2 = (const bf16_t*)d_in[8];
  float* ws = (float*)d_ws;
  float* q = ws; float* po = ws + 4096; float* pml = po + 32 * 32 * 128; float* hp = pml + 2048; float* hbuf = hp + 32 * 512; float* hm = hbuf + 512;
  k_q<<<64, 256, 0, stream>>>(x, wq, q);
  k_attn<<<256, 256, 0, stream>>>(q, kc, vc, po, pml);
  k_wo<<<256, 256, 0, stream>>>(po, pml, wo, hp);
  k_up<<<28, 256, 0, stream>>>(x, hp, w1, w3, hbuf, hm);
  k_down<<<8, 256, 0, stream>>>(hbuf, hm, w2, (__hip_bfloat16*)d_out);
}
```

```cpp
#include <hip/hip_runtime.h>
#include <stdint.h>

#define GAS __attribute__((address_space(1)))
typedef unsigned short bf16_t;
typedef unsigned v4u __attribute__((ext_vector_type(4)));
typedef unsigned v2u __attribute__((ext_vector_type(2)));
typedef float f4 __attribute__((ext_vector_type(4)));
typedef float f2 __attribute__((ext_vector_type(2)));
typedef short s8 __attribute__((ext_vector_type(8)));
typedef __bf16 bf2 __attribute__((ext_vector_type(2)));

constexpr int D = 512, SK = 65536, DH = 128, WIN = 4096, F = 1792;
constexpr int NTHR = 512;
constexpr int WS_E = 0;
constexpr int WS_Q = 4096;
constexpr int REC = 2176;
constexpr int WS_B = WS_Q + 65536;
constexpr int WS_C = WS_B + 128 * REC;
constexpr int WS_D = WS_C + 16384;
constexpr int WS_END = WS_D + 56 * 2048;
constexpr int OOB = 0x7ff00000;
constexpr int SPIN_CAP = 1 << 17;
#define DELAY_WO 300
#define DELAY_UP 380

#define STAMP(i)

__device__ __forceinline__ v4u ldnt(const bf16_t* p) { return __builtin_nontemporal_load((const GAS v4u*)p); }
__device__ __forceinline__ v4u ldg(__amdgpu_buffer_rsrc_t rs, int off) { return __builtin_amdgcn_raw_buffer_load_b128(rs, off, 0, 16); }
__device__ __forceinline__ void stg(__amdgpu_buffer_rsrc_t rs, int off, v4u v) { __builtin_amdgcn_raw_buffer_store_b128(v, rs, off, 0, 16); }
__device__ __forceinline__ unsigned pk(float lo, float hi) { f2 v = {lo, hi}; return __builtin_bit_cast(unsigned, __builtin_convertvector(v, bf2)); }
__device__ __forceinline__ float blo(unsigned u) { return __uint_as_float(u << 16); }
__device__ __forceinline__ float bhi(unsigned u) { return __uint_as_float(u & 0xffff0000u); }
__device__ __forceinline__ float rbf(float f) { return blo(pk(f, f)); }
__device__ __forceinline__ f4 mfma(v4u a, v4u b, f4 c) {
  return __builtin_amdgcn_mfma_f32_16x16x32_bf16(__builtin_bit_cast(s8, a), __builtin_bit_cast(s8, b), c, 0, 0, 0);
}
template <int CTRL> __device__ __forceinline__ float dpp(float x) {
  return __builtin_bit_cast(float, __builtin_amdgcn_mov_dpp(__builtin_bit_cast(int, x), CTRL, 0xf, 0xf, true));
}
template <int CTRL> __device__ __forceinline__ unsigned dppu(unsigned x) { return (unsigned)__builtin_amdgcn_mov_dpp((int)x, CTRL, 0xf, 0xf, true); }
constexpr int XOR1 = 0xB1, XOR2 = 0x4E, HMIR = 0x141, ROR4 = 0x124, ROR8 = 0x128, ROR10 = 0x12A, ROR12 = 0x12C, ROR14 = 0x12E;
__device__ __forceinline__ float xg_sum(float x) {
  auto s = __builtin_amdgcn_permlane16_swap(__float_as_uint(x), __float_as_uint(x), false, false);
  x = __uint_as_float(s[0]) + __uint_as_float(s[1]);
  auto t = __builtin_amdgcn_permlane32_swap(__float_as_uint(x), __float_as_uint(x), false, false);
  return __uint_as_float(t[0]) + __uint_as_float(t[1]);
}
__device__ __forceinline__ float xg_max(float x) {
  auto s = __builtin_amdgcn_permlane16_swap(__float_as_uint(x), __float_as_uint(x), false, false);
  x = fmaxf(__uint_as_float(s[0]), __uint_as_float(s[1]));
  auto t = __builtin_amdgcn_permlane32_swap(__float_as_uint(x), __float_as_uint(x), false, false);
  return fmaxf(__uint_as_float(t[0]), __uint_as_float(t[1]));
}
__device__ __forceinline__ float row_sum(float x) { x += dpp<XOR1>(x); x += dpp<XOR2>(x); x += dpp<ROR4>(x); x += dpp<ROR8>(x); return x; }
__device__ __forceinline__ float row_max(float x) {
  x = fmaxf(x, dpp<XOR1>(x)); x = fmaxf(x, dpp<XOR2>(x)); x = fmaxf(x, dpp<ROR4>(x)); x = fmaxf(x, dpp<ROR8>(x)); return x;
}
__device__ __forceinline__ void transpose8(const v4u (&w)[8], v4u (&t)[8]) {
#pragma unroll
  for (int e = 0; e < 8; ++e)
#pragma unroll
    for (int jj = 0; jj < 4; ++jj)
      t[e][jj] = __builtin_amdgcn_perm(w[2 * jj + 1][e >> 1], w[2 * jj][e >> 1], (e & 1) ? 0x07060302u : 0x05040100u);
}
__device__ __forceinline__ void delay_until(unsigned long long t0, int ticks) {
  if (ticks > 0) while ((long long)(__builtin_amdgcn_s_memrealtime() - t0) < ticks) __builtin_amdgcn_s_sleep(2);
}
#define MEMFENCE() asm volatile("" ::: "memory")

__device__ __forceinline__ bool tags_ok(v4u a, unsigned tag) { return (a.y == tag) & (a.w == tag); }

__global__ void __launch_bounds__(NTHR, 2)
fused_decode(const bf16_t* __restrict__ x, const bf16_t* __restrict__ kc, const bf16_t* __restrict__ vc, const bf16_t* __restrict__ wq,
             const bf16_t* __restrict__ wo, const bf16_t* __restrict__ w1, const bf16_t* __restrict__ w3, const bf16_t* __restrict__ w2,
             bf16_t* __restrict__ out, unsigned char* __restrict__ ws) {
  __shared__ __attribute__((aligned(16))) float lds[21504];
  STAMP(0);
  const int b = blockIdx.x, xl = b & 7, r = b >> 3;
  const int tid = threadIdx.x, l = tid & 63, w = __builtin_amdgcn_readfirstlane(tid >> 6);
  const int c = l & 15, g4 = l >> 4;
  const __amdgpu_buffer_rsrc_t rs = __builtin_amdgcn_make_buffer_rsrc(ws, 0, WS_END, 0x00020000);
  const unsigned epoch_v = __builtin_amdgcn_raw_buffer_load_b32(rs, WS_E, 0, 16);
  constexpr float LOG2E = 1.4426950408889634f;
  const f4 Z4 = {0.f, 0.f, 0.f, 0.f};

  if (r < 16) {
    const int g = xl, s = r;
    const size_t base = ((size_t)g * SK + (SK - WIN) + s * 256 + w * 32) * DH;
    v4u K[8], V[8], Vt[8];
    {
      const bf16_t* kb = kc + base + (c >> 2) * DH + 32 * g4 + 8 * (c & 3);
#pragma unroll
      for (int i = 0; i < 8; ++i) K[i] = ldnt(kb + i * 4 * DH);
      const bf16_t* vb = vc + base + g4 * DH + 8 * c;
#pragma unroll
      for (int j = 0; j < 8; ++j) V[j] = ldnt(vb + j * 4 * DH);
    }
    __builtin_amdgcn_sched_barrier(0);
    const unsigned epoch = __builtin_amdgcn_readfirstlane(epoch_v);
    const unsigned tag = epoch * 0x9E3779B1u + 0x7F4A7C15u;
    STAMP(1);
    {
      const int off = WS_Q + g * 8192 + (l >> 5) * 4096 + w * 512 + (l & 31) * 16;
      v4u a;
      for (int spins = 0; spins < SPIN_CAP; ++spins) {
        MEMFENCE();
        a = ldg(rs, off);
        if (__all(tags_ok(a, tag))) break;
      }
      auto t0 = __builtin_amdgcn_permlane32_swap(a.x, a.x, false, false);
      auto t1 = __builtin_amdgcn_permlane32_swap(a.z, a.z, false, false);
      const unsigned qq = pk(__uint_as_float(t0[0]) + __uint_as_float(t0[1]), __uint_as_float(t1[0]) + __uint_as_float(t1[1]));
      if (l < 32) ((unsigned*)lds)[4160 + w * 32 + l] = qq;
    }
    transpose8(V, Vt);
    __syncthreads();
    const v4u Q = *(const v4u*)(lds + 4160 + (c >> 2) * 64 + g4 * 16 + (c & 3) * 4);
    STAMP(2);
    float sc[8];
#pragma unroll
    for (int i = 0; i < 8; ++i) {
      const f4 z = mfma(K[i], Q, Z4);
      const float t0 = (c & 1) ? z[1] : z[0], t1 = (c & 1) ? z[3] : z[2];
      float v = (c & 2) ? t1 : t0;
      v += dpp<XOR1>(v); v += dpp<XOR2>(v);
      sc[i] = rbf(rbf(v) * 0.08838834764831845f);
    }
    float m = fmaxf(fmaxf(fmaxf(sc[0], sc[1]), fmaxf(sc[2], sc[3])), fmaxf(fmaxf(sc[4], sc[5]), fmaxf(sc[6], sc[7])));
    m = xg_max(m);
    float p[8], ls = 0.f;
#pragma unroll
    for (int i = 0; i < 8; ++i) { p[i] = __builtin_amdgcn_exp2f((sc[i] - m) * LOG2E); ls += p[i]; }
    const v4u P = {pk(p[0], p[1]), pk(p[2], p[3]), pk(p[4], p[5]), pk(p[6], p[7])};
    float o[8];
#pragma unroll
    for (int e = 0; e < 8; ++e) o[e] = mfma(P, Vt[e], Z4)[0];
    ls = xg_sum(ls);
    float* Ol = lds + w * 512 + g4 * 128 + c * 8;
    *(f4*)Ol = f4{o[0], o[1], o[2], o[3]};
    *(f4*)(Ol + 4) = f4{o[4], o[5], o[6], o[7]};
    if (l < 16 && (c & 3) == 0) { lds[4096 + (c >> 2) * 8 + w] = m; lds[4128 + (c >> 2) * 8 + w] = ls; }
    __syncthreads();
    STAMP(3);
    if (w < 2) {
      const int h = tid >> 5, d4 = tid & 31;
      const f4 m0 = *(const f4*)(lds + 4096 + h * 8), m1 = *(const f4*)(lds + 4096 + h * 8 + 4);
      const f4 l0 = *(const f4*)(lds + 4128 + h * 8), l1 = *(const f4*)(lds + 4128 + h * 8 + 4);
      const float M = fmaxf(fmaxf(fmaxf(m0[0], m0[1]), fmaxf(m0[2], m0[3])), fmaxf(fmaxf(m1[0], m1[1]), fmaxf(m1[2], m1[3])));
      f4 acc = Z4;
      float L = 0.f;
#pragma unroll
      for (int i = 0; i < 8; ++i) {
        const float wt = __builtin_amdgcn_exp2f(((i < 4 ? m0[i & 3] : m1[i & 3]) - M) * LOG2E);
        L += wt * (i < 4 ? l0[i & 3] : l1[i & 3]);
        acc += wt * *(const f4*)(lds + i * 512 + h * 128 + d4 * 4);
      }
      const float inv = __builtin_amdgcn_rcpf(L);
      const int rec = WS_B + (g * 16 + s) * REC;
      stg(rs, rec + h * 512 + d4 * 16, v4u{pk(acc[0] * inv, acc[1] * inv), tag, pk(acc[2] * inv, acc[3] * inv), tag});
      stg(rs, d4 == 0 ? rec + 2048 + h * 16 : OOB, v4u{__float_as_uint(M), tag, __float_as_uint(L), tag});
    }
    STAMP(4);
    if (xl < 4 && r < 8 && w == 2) {
      const int d = xl * 8 + r;
      const v2u xv = *(const GAS v2u*)(x + 16 * d + 4 * (l & 3));
      v4u a;
      {
        const int off = l < 32 ? WS_C + (l >> 2) * 2048 + d * 64 + (l & 3) * 16 : OOB;
        for (int spins = 0; spins < SPIN_CAP; ++spins) {
          MEMFENCE();
          a = ldg(rs, off);
          if (__all(tags_ok(a, tag) || l >= 32)) break;
        }
      }
      float hv[4] = {blo(a.x), bhi(a.x), blo(a.z), bhi(a.z)};
      const float xf[4] = {blo(xv.x), bhi(xv.x), blo(xv.y), bhi(xv.y)};
#pragma unroll
      for (int i = 0; i < 4; ++i) {
        hv[i] += dpp<ROR4>(hv[i]); hv[i] += dpp<ROR8>(hv[i]); hv[i] = xg_sum(hv[i]);
        hv[i] = rbf(xf[i] + rbf(hv[i]));
      }
      v4u y[4];
      {
        const int off = WS_D + (l >> 2) * 2048 + d * 64 + (l & 3) * 16;
        for (int spins = 0; spins < SPIN_CAP; ++spins) {
          MEMFENCE();
#pragma unroll
          for (int i = 0; i < 4; ++i) y[i] = ldg(rs, off + i * 32768);
          if (__all(tags_ok(y[0], tag) & tags_ok(y[1], tag) & tags_ok(y[2], tag) & (tags_ok(y[3], tag) || l >= 32))) break;
        }
      }
      STAMP(5);
      float v[4];
      v[0] = (blo(y[0].x) + blo(y[1].x)) + (blo(y[2].x) + blo(y[3].x));
      v[1] = (bhi(y[0].x) + bhi(y[1].x)) + (bhi(y[2].x) + bhi(y[3].x));
      v[2] = (blo(y[0].z) + blo(y[1].z)) + (blo(y[2].z) + blo(y[3].z));
      v[3] = (bhi(y[0].z) + bhi(y[1].z)) + (bhi(y[2].z) + bhi(y[3].z));
#pragma unroll
      for (int i = 0; i < 4; ++i) {
        v[i] += dpp<ROR4>(v[i]); v[i] += dpp<ROR8>(v[i]); v[i] = xg_sum(v[i]);
        v[i] = hv[i] + rbf(v[i]);
      }
      if (l < 4) *(GAS v2u*)(out + 16 * d + 4 * l) = v2u{pk(v[0], v[1]), pk(v[2], v[3])};
      if (d == 0 && l == 0) __builtin_amdgcn_raw_buffer_store_b32(epoch + 1u, rs, WS_E, 0, 16);
      STAMP(6);
    }
  } else {
    const int rr = r - 16, qi = xl * 8 + (rr >> 1), kh = rr & 1;
    const unsigned long long t0 = (DELAY_WO > 0 || DELAY_UP > 0) ? __builtin_amdgcn_s_memrealtime() : 0ull;
    const int row0 = 64 * w + 16 * g4;
    v4u Wr[8], Wt[8];
    if (w < 4) {
      const v4u xa = *(const GAS v4u*)(x + 8 * l);
      const v4u xb = *(const GAS v4u*)(x + 256 * kh + row0 + 8 * ((c >> 1) & 1));
      const bf16_t* wb = wq + (size_t)(256 * kh + row0 + 8 * (c >> 3)) * 4096 + 64 * qi + 8 * (c & 7);
#pragma unroll
      for (int j = 0; j < 8; ++j) Wr[j] = ldnt(wb + j * 4096);
      __builtin_amdgcn_sched_barrier(0);
      float xs[8] = {blo(xa.x), bhi(xa.x), blo(xa.y), bhi(xa.y), blo(xa.z), bhi(xa.z), blo(xa.w), bhi(xa.w)};
      float su = 0.f;
#pragma unroll
      for (int i = 0; i < 8; ++i) su += xs[i];
      const float mean = xg_sum(row_sum(su)) * (1.f / D);
      float sq = 0.f;
#pragma unroll
      for (int i = 0; i < 8; ++i) { const float dd = xs[i] - mean; sq += dd * dd; }
      const float var = xg_sum(row_sum(sq)) * (1.f / D);
      const float mb = rbf(mean), den = rbf(sqrtf(rbf(rbf(var) + 1e-5f)));
      v4u A;
#pragma unroll
      for (int i = 0; i < 4; ++i) A[i] = pk(rbf(blo(xb[i]) - mb) / den, rbf(bhi(xb[i]) - mb) / den);
      transpose8(Wr, Wt);
      float y[8];
#pragma unroll
      for (int e = 0; e < 8; ++e) { const f4 z = mfma(A, Wt[e], Z4); y[e] = (c & 8) ? z[2] : z[0]; }
      float* pq = lds + 3072 + (w * 2 + (c >> 3)) * 64 + (c & 7) * 8;
      if (l < 16) { *(f4*)pq = f4{y[0], y[1], y[2], y[3]}; *(f4*)(pq + 4) = f4{y[4], y[5], y[6], y[7]}; }
    }
    const unsigned epoch = __builtin_amdgcn_readfirstlane(epoch_v);
    const unsigned tag = epoch * 0x9E3779B1u + 0x7F4A7C15u;
    __syncthreads();
    STAMP(1);
    if (w == 0) {
      const float* pr = lds + 3072 + (l >> 5) * 256 + (l & 31) * 2;
      f2 acc = (*(const f2*)pr + *(const f2*)(pr + 64)) + (*(const f2*)(pr + 128) + *(const f2*)(pr + 192));
#pragma unroll
      for (int i = 0; i < 2; ++i) {
        auto t = __builtin_amdgcn_permlane32_swap(__float_as_uint(acc[i]), __float_as_uint(acc[i]), false, false);
        acc[i] = __uint_as_float(t[0]) + __uint_as_float(t[1]);
      }
      stg(rs, l < 32 ? WS_Q + xl * 8192 + kh * 4096 + (rr >> 1) * 512 + l * 16 : OOB, v4u{__float_as_uint(acc[0]), tag, __float_as_uint(acc[1]), tag});
    }
    STAMP(2);
    if (xl >= 4) {
    float* pl = lds + (w * 2 + (c >> 3)) * 64 + (c & 7) * 8;
    const int g = (xl - 4) * 2 + (rr >> 3), cb = rr & 7;
    delay_until(t0, DELAY_WO);
    {
      const bf16_t* wb = wo + (size_t)(512 * g + row0 + 8 * (c >> 3)) * D + 64 * cb + 8 * (c & 7);
#pragma unroll
      for (int j = 0; j < 8; ++j) Wr[j] = ldnt(wb + j * D);
      transpose8(Wr, Wt);
    }
    STAMP(3);
    v4u a[4], ml;
    {
      const int off = WS_B + (g * 16 + (c >> 2)) * REC + row0 * 4 + (c & 3) * 16;
      const int offm = WS_B + (g * 16 + (l >> 2)) * REC + 2048 + (w >> 1) * 16;
      for (int spins = 0; spins < SPIN_CAP; ++spins) {
        MEMFENCE();
#pragma unroll
        for (int i = 0; i < 4; ++i) a[i] = ldg(rs, off + i * 4 * REC);
        ml = ldg(rs, offm);
        if (__all(tags_ok(a[0], tag) & tags_ok(a[1], tag) & tags_ok(a[2], tag) & tags_ok(a[3], tag) & tags_ok(ml, tag))) break;
      }
    }
    STAMP(4);
    {
      const float ms = __uint_as_float(ml.x);
      float M = fmaxf(ms, dpp<ROR4>(ms)); M = fmaxf(M, dpp<ROR8>(M)); M = xg_max(M);
      const float wt = __builtin_amdgcn_exp2f((ms - M) * LOG2E) * __uint_as_float(ml.z);
      auto t = __builtin_amdgcn_permlane16_swap(__float_as_uint(wt), __float_as_uint(wt), false, false);
      auto t02 = __builtin_amdgcn_permlane32_swap(t[0], t[0], false, false);
      auto t13 = __builtin_amdgcn_permlane32_swap(t[1], t[1], false, false);
      const float w0 = __uint_as_float(t02[0]), w1_ = __uint_as_float(t13[0]), w2_ = __uint_as_float(t02[1]), w3_ = __uint_as_float(t13[1]);
      float L = (w0 + w1_) + (w2_ + w3_);
      float o[4];
      o[0] = (w0 * blo(a[0].x) + w1_ * blo(a[1].x)) + (w2_ * blo(a[2].x) + w3_ * blo(a[3].x));
      o[1] = (w0 * bhi(a[0].x) + w1_ * bhi(a[1].x)) + (w2_ * bhi(a[2].x) + w3_ * bhi(a[3].x));
      o[2] = (w0 * blo(a[0].z) + w1_ * blo(a[1].z)) + (w2_ * blo(a[2].z) + w3_ * blo(a[3].z));
      o[3] = (w0 * bhi(a[0].z) + w1_ * bhi(a[1].z)) + (w2_ * bhi(a[2].z) + w3_ * bhi(a[3].z));
      L += dpp<ROR4>(L); L += dpp<ROR8>(L);
      const float inv = __builtin_amdgcn_rcpf(L);
#pragma unroll
      for (int i = 0; i < 4; ++i) { o[i] += dpp<ROR4>(o[i]); o[i] += dpp<ROR8>(o[i]); o[i] *= inv; }
      const unsigned p01 = pk(o[0], o[1]), p23 = pk(o[2], o[3]);
      const v4u A = {p01, p23, dppu<XOR1>(p01), dppu<XOR1>(p23)};
      float y[8];
#pragma unroll
      for (int e = 0; e < 8; ++e) { const f4 z = mfma(A, Wt[e], Z4); y[e] = (c & 8) ? z[2] : z[0]; }
      if (l < 16) { *(f4*)(pl + 1024) = f4{y[0], y[1], y[2], y[3]}; *(f4*)(pl + 1028) = f4{y[4], y[5], y[6], y[7]}; }
    }
    __syncthreads();
    STAMP(5);
    if (w == 0) {
      const float* pr = lds + 1024 + g4 * 256 + c * 4;
      f4 acc = (*(const f4*)pr + *(const f4*)(pr + 64)) + (*(const f4*)(pr + 128) + *(const f4*)(pr + 192));
#pragma unroll
      for (int i = 0; i < 4; ++i) acc[i] = xg_sum(acc[i]);
      stg(rs, l < 16 ? WS_C + g * 2048 + cb * 256 + c * 16 : OOB, v4u{pk(acc[0], acc[1]), tag, pk(acc[2], acc[3]), tag});
    }
    STAMP(6);
    } else if (r < 30) {
    const int u = xl * 14 + (r - 16);
    delay_until(t0, DELAY_UP);
    v4u W2r[8], W2t[8];
    const bf16_t* wb = ((c & 4) ? w3 : w1) + (size_t)(row0 + 8 * (c >> 3)) * F + 32 * u + 8 * (c & 3);
#pragma unroll
    for (int j = 0; j < 8; ++j) Wr[j] = ldnt(wb + j * F);
    const v2u xv = *(const GAS v2u*)(x + row0 + 4 * (c & 3));
    if (w < 4) {
      const bf16_t* w2b = w2 + (size_t)(32 * u + 8 * g4) * D + 128 * w + 8 * c;
#pragma unroll
      for (int j = 0; j < 8; ++j) W2r[j] = ldnt(w2b + j * D);
    }
    __builtin_amdgcn_sched_barrier(0);
    transpose8(Wr, Wt);
    float* pl = lds + (w * 2 + (c >> 3)) * 64 + (c & 3) * 16 + ((c >> 2) & 1) * 8;
    {
      const v4u ONE = {0x3f803f80u, 0x3f803f80u, 0x3f803f80u, 0x3f803f80u};
      float y[8];
#pragma unroll
      for (int e = 0; e < 8; ++e) y[e] = mfma(ONE, Wt[e], Z4)[0];
      if (l < 16) { *(f4*)(pl + 1024) = f4{y[0], y[1], y[2], y[3]}; *(f4*)(pl + 1028) = f4{y[4], y[5], y[6], y[7]}; }
    }
    __syncthreads();
    float cs = 0.f;
    if (w < 4) {
#pragma unroll
      for (int i = 0; i < 16; ++i) cs += lds[1024 + i * 64 + l];
      transpose8(W2r, W2t);
    }
    STAMP(3);
    v4u a0, a1;
    {
      const int off = WS_C + (c >> 2) * 2048 + row0 * 4 + (c & 3) * 16;
      for (int spins = 0; spins < SPIN_CAP; ++spins) {
        MEMFENCE();
        a0 = ldg(rs, off); a1 = ldg(rs, off + 8192);
        if (__all(tags_ok(a0, tag) & tags_ok(a1, tag))) break;
      }
    }
    STAMP(4);
    float h[4] = {blo(a0.x) + blo(a1.x), bhi(a0.x) + bhi(a1.x), blo(a0.z) + blo(a1.z), bhi(a0.z) + bhi(a1.z)};
#pragma unroll
    for (int i = 0; i < 4; ++i) { h[i] += dpp<ROR4>(h[i]); h[i] += dpp<ROR8>(h[i]); }
    const unsigned h01 = pk(blo(xv.x) + rbf(h[0]), bhi(xv.x) + rbf(h[1])), h23 = pk(blo(xv.y) + rbf(h[2]), bhi(xv.y) + rbf(h[3]));
    {
      const v4u A = {h01, h23, dppu<XOR1>(h01), dppu<XOR1>(h23)};
      float y[8];
#pragma unroll
      for (int e = 0; e < 8; ++e) { const f4 z = mfma(A, Wt[e], Z4); y[e] = (c & 8) ? z[2] : z[0]; }
      h[0] = blo(h01); h[1] = bhi(h01); h[2] = blo(h23); h[3] = bhi(h23);
      float su = (h[0] + h[1]) + (h[2] + h[3]);
      su += dpp<XOR1>(su); su += dpp<XOR2>(su); su = xg_sum(su);
      const float mw = su * (1.f / 64);
      float sq = 0.f;
#pragma unroll
      for (int i = 0; i < 4; ++i) { const float dd = h[i] - mw; sq += dd * dd; }
      sq += dpp<XOR1>(sq); sq += dpp<XOR2>(sq); sq = xg_sum(sq);
      if (l < 16) { *(f4*)pl = f4{y[0], y[1], y[2], y[3]}; *(f4*)(pl + 4) = f4{y[4], y[5], y[6], y[7]}; }
      if (l == 0) { lds[2048 + w] = mw; lds[2056 + w] = sq; }
    }
    __syncthreads();
    STAMP(5);
    if (w < 4) {
      float acc = 0.f;
#pragma unroll
      for (int i = 0; i < 16; ++i) acc += lds[i * 64 + l];
      const f4 m0 = *(const f4*)(lds + 2048), m1 = *(const f4*)(lds + 2052), q0 = *(const f4*)(lds + 2056), q1 = *(const f4*)(lds + 2060);
      const float mean = (((m0[0] + m0[1]) + (m0[2] + m0[3])) + ((m1[0] + m1[1]) + (m1[2] + m1[3]))) * 0.125f;
      const float m2 = ((q0[0] + q0[1]) + (q0[2] + q0[3])) + ((q1[0] + q1[1]) + (q1[2] + q1[3]));
      float dv = 0.f;
#pragma unroll
      for (int i = 0; i < 4; ++i) { const float d0 = m0[i] - mean, d1 = m1[i] - mean; dv += d0 * d0 + d1 * d1; }
      const float var = (m2 + 64.f * dv) * (1.f / D);
      const float mb = rbf(mean), den = rbf(sqrtf(rbf(rbf(var) + 1e-5f)));
      const float val = rbf((acc - mb * cs) / den);
      const float oth = dpp<ROR8>(val);
      const float gt = (c & 8) ? oth : val, up = (c & 8) ? val : oth;
      const float si = rbf(gt * __builtin_amdgcn_rcpf(1.f + __builtin_amdgcn_exp2f(-gt * LOG2E)));
      const float hm = si * up;
      const unsigned p = pk(hm, dpp<XOR1>(hm));
      const v4u A = {p, dppu<ROR14>(p), dppu<ROR12>(p), dppu<ROR10>(p)};
      float y[8];
#pragma unroll
      for (int e = 0; e < 8; ++e) y[e] = mfma(A, W2t[e], Z4)[0];
      const unsigned s0 = l < 32 ? pk(y[0], y[1]) : pk(y[4], y[5]), s1 = l < 32 ? pk(y[2], y[3]) : pk(y[6], y[7]);
      stg(rs, (g4 & 1) ? OOB : WS_D + u * 2048 + w * 512 + c * 32 + (l >> 5) * 16, v4u{s0, tag, s1, tag});
    }
    STAMP(6);
    }
  }
}

extern "C" void kernel_launch(void* const* d_in, const int* in_sizes, int n_in, void* d_out, int out_size, void* d_ws, size_t ws_size,
                              hipStream_t stream) {
  fused_decode<<<256, NTHR, 0, stream>>>((const bf16_t*)d_in[0], (const bf16_t*)d_in[1], (const bf16_t*)d_in[2], (const bf16_t*)d_in[3],
                                         (const bf16_t*)d_in[4], (const bf16_t*)d_in[6], (const bf16_t*)d_in[7], (const bf16_t*)d_in[8],
                                         (bf16_t*)d_out, (unsigned char*)d_ws);
}
```

```cpp
#include <hip/hip_runtime.h>
#include <stdint.h>

#define GAS __attribute__((address_space(1)))
typedef unsigned short bf16_t;
typedef unsigned v4u __attribute__((ext_vector_type(4)));
typedef unsigned v2u __attribute__((ext_vector_type(2)));
typedef float f4 __attribute__((ext_vector_type(4)));
typedef float f2 __attribute__((ext_vector_type(2)));
typedef short s8 __attribute__((ext_vector_type(8)));
typedef __bf16 bf2 __attribute__((ext_vector_type(2)));

constexpr int D = 512, SK = 65536, DH = 128, WIN = 4096, F = 1792;
constexpr int NTHR = 512;
constexpr int WS_E = 0;
constexpr int WS_Q = 4096;
constexpr int REC = 2176;
constexpr int WS_B = WS_Q + 65536;
constexpr int WS_C = WS_B + 128 * REC;
constexpr int WS_D = WS_C + 16384;
constexpr int WS_END = WS_D + 56 * 2048;
constexpr int OOB = 0x7ff00000;
constexpr int SPIN_CAP = 1 << 17;
#define DELAY_WO 300
#define DELAY_UP 380

#define STAMP(i) do { __builtin_amdgcn_sched_barrier(0); st[i] = __builtin_amdgcn_s_memrealtime(); __builtin_amdgcn_sched_barrier(0); } while (0)

__device__ __forceinline__ v4u ldnt(const bf16_t* p) { return __builtin_nontemporal_load((const GAS v4u*)p); }
__device__ __forceinline__ v4u ldg(__amdgpu_buffer_rsrc_t rs, int off) { return __builtin_amdgcn_raw_buffer_load_b128(rs, off, 0, 16); }
__device__ __forceinline__ void stg(__amdgpu_buffer_rsrc_t rs, int off, v4u v) { __builtin_amdgcn_raw_buffer_store_b128(v, rs, off, 0, 16); }
__device__ __forceinline__ unsigned pk(float lo, float hi) { f2 v = {lo, hi}; return __builtin_bit_cast(unsigned, __builtin_convertvector(v, bf2)); }
__device__ __forceinline__ float blo(unsigned u) { return __uint_as_float(u << 16); }
__device__ __forceinline__ float bhi(unsigned u) { return __uint_as_float(u & 0xffff0000u); }
__device__ __forceinline__ float rbf(float f) { return blo(pk(f, f)); }
__device__ __forceinline__ f4 mfma(v4u a, v4u b, f4 c) {
  return __builtin_amdgcn_mfma_f32_16x16x32_bf16(__builtin_bit_cast(s8, a), __builtin_bit_cast(s8, b), c, 0, 0, 0);
}
template <int CTRL> __device__ __forceinline__ float dpp(float x) {
  return __builtin_bit_cast(float, __builtin_amdgcn_mov_dpp(__builtin_bit_cast(int, x), CTRL, 0xf, 0xf, true));
}
template <int CTRL> __device__ __forceinline__ unsigned dppu(unsigned x) { return (unsigned)__builtin_amdgcn_mov_dpp((int)x, CTRL, 0xf, 0xf, true); }
constexpr int XOR1 = 0xB1, XOR2 = 0x4E, HMIR = 0x141, ROR4 = 0x124, ROR8 = 0x128, ROR10 = 0x12A, ROR12 = 0x12C, ROR14 = 0x12E;
__device__ __forceinline__ float xg_sum(float x) {
  auto s = __builtin_amdgcn_permlane16_swap(__float_as_uint(x), __float_as_uint(x), false, false);
  x = __uint_as_float(s[0]) + __uint_as_float(s[1]);
  auto t = __builtin_amdgcn_permlane32_swap(__float_as_uint(x), __float_as_uint(x), false, false);
  return __uint_as_float(t[0]) + __uint_as_float(t[1]);
}
__device__ __forceinline__ float xg_max(float x) {
  auto s = __builtin_amdgcn_permlane16_swap(__float_as_uint(x), __float_as_uint(x), false, false);
  x = fmaxf(__uint_as_float(s[0]), __uint_as_float(s[1]));
  auto t = __builtin_amdgcn_permlane32_swap(__float_as_uint(x), __float_as_uint(x), false, false);
  return fmaxf(__uint_as_float(t[0]), __uint_as_float(t[1]));
}
__device__ __forceinline__ float row_sum(float x) { x += dpp<XOR1>(x); x += dpp<XOR2>(x); x += dpp<ROR4>(x); x += dpp<ROR8>(x); return x; }
__device__ __forceinline__ float row_max(float x) {
  x = fmaxf(x, dpp<XOR1>(x)); x = fmaxf(x, dpp<XOR2>(x)); x = fmaxf(x, dpp<ROR4>(x)); x = fmaxf(x, dpp<ROR8>(x)); return x;
}
__device__ __forceinline__ void transpose8(const v4u (&w)[8], v4u (&t)[8]) {
#pragma unroll
  for (int e = 0; e < 8; ++e)
#pragma unroll
    for (int jj = 0; jj < 4; ++jj)
      t[e][jj] = __builtin_amdgcn_perm(w[2 * jj + 1][e >> 1], w[2 * jj][e >> 1], (e & 1) ? 0x07060302u : 0x05040100u);
}
__device__ __forceinline__ void delay_until(unsigned long long t0, int ticks) {
  if (ticks > 0) while ((long long)(__builtin_amdgcn_s_memrealtime() - t0) < ticks) __builtin_amdgcn_s_sleep(2);
}
#define MEMFENCE() asm volatile("" ::: "memory")

__device__ __forceinline__ bool tags_ok(v4u a, unsigned tag) { return (a.y == tag) & (a.w == tag); }

__global__ void __launch_bounds__(NTHR, 2)
fused_decode(const bf16_t* __restrict__ x, const bf16_t* __restrict__ kc, const bf16_t* __restrict__ vc, const bf16_t* __restrict__ wq,
             const bf16_t* __restrict__ wo, const bf16_t* __restrict__ w1, const bf16_t* __restrict__ w3, const bf16_t* __restrict__ w2,
             bf16_t* __restrict__ out, unsigned char* __restrict__ ws) {
  __shared__ __attribute__((aligned(16))) float lds[21504];
  unsigned long long st[8] = {0, 0, 0, 0, 0, 0, 0, 0};
  STAMP(0);
  const int b = blockIdx.x, xl = b & 7, r = b >> 3;
  const int tid = threadIdx.x, l = tid & 63, w = __builtin_amdgcn_readfirstlane(tid >> 6);
  const int c = l & 15, g4 = l >> 4;
  const __amdgpu_buffer_rsrc_t rs = __builtin_amdgcn_make_buffer_rsrc(ws, 0, WS_END, 0x00020000);
  const unsigned epoch_v = __builtin_amdgcn_raw_buffer_load_b32(rs, WS_E, 0, 16);
  constexpr float LOG2E = 1.4426950408889634f;
  const f4 Z4 = {0.f, 0.f, 0.f, 0.f};

  if (r < 16) {
    const int g = xl, s = r;
    const size_t base = ((size_t)g * SK + (SK - WIN) + s * 256 + w * 32) * DH;
    v4u K[8], V[8], Vt[8];
    {
      const bf16_t* kb = kc + base + (c >> 2) * DH + 32 * g4 + 8 * (c & 3);
#pragma unroll
      for (int i = 0; i < 8; ++i) K[i] = ldnt(kb + i * 4 * DH);
      const bf16_t* vb = vc + base + g4 * DH + 8 * c;
#pragma unroll
      for (int j = 0; j < 8; ++j) V[j] = ldnt(vb + j * 4 * DH);
    }
    __builtin_amdgcn_sched_barrier(0);
    const unsigned epoch = __builtin_amdgcn_readfirstlane(epoch_v);
    const unsigned tag = epoch * 0x9E3779B1u + 0x7F4A7C15u;
    STAMP(1);
    {
      const int off = WS_Q + g * 8192 + (l >> 5) * 4096 + w * 512 + (l & 31) * 16;
      v4u a;
      for (int spins = 0; spins < SPIN_CAP; ++spins) {
        MEMFENCE();
        a = ldg(rs, off);
        if (__all(tags_ok(a, tag))) break;
      }
      auto t0 = __builtin_amdgcn_permlane32_swap(a.x, a.x, false, false);
      auto t1 = __builtin_amdgcn_permlane32_swap(a.z, a.z, false, false);
      const unsigned qq = pk(__uint_as_float(t0[0]) + __uint_as_float(t0[1]), __uint_as_float(t1[0]) + __uint_as_float(t1[1]));
      if (l < 32) ((unsigned*)lds)[4160 + w * 32 + l] = qq;
    }
    transpose8(V, Vt);
    __syncthreads();
    const v4u Q = *(const v4u*)(lds + 4160 + (c >> 2) * 64 + g4 * 16 + (c & 3) * 4);
    STAMP(2);
    float sc[8];
#pragma unroll
    for (int i = 0; i < 8; ++i) {
      const f4 z = mfma(K[i], Q, Z4);
      const float t0 = (c & 1) ? z[1] : z[0], t1 = (c & 1) ? z[3] : z[2];
      float v = (c & 2) ? t1 : t0;
      v += dpp<XOR1>(v); v += dpp<XOR2>(v);
      sc[i] = rbf(rbf(v) * 0.08838834764831845f);
    }
    float m = fmaxf(fmaxf(fmaxf(sc[0], sc[1]), fmaxf(sc[2], sc[3])), fmaxf(fmaxf(sc[4], sc[5]), fmaxf(sc[6], sc[7])));
    m = xg_max(m);
    float p[8], ls = 0.f;
#pragma unroll
    for (int i = 0; i < 8; ++i) { p[i] = __builtin_amdgcn_exp2f((sc[i] - m) * LOG2E); ls += p[i]; }
    const v4u P = {pk(p[0], p[1]), pk(p[2], p[3]), pk(p[4], p[5]), pk(p[6], p[7])};
    float o[8];
#pragma unroll
    for (int e = 0; e < 8; ++e) o[e] = mfma(P, Vt[e], Z4)[0];
    ls = xg_sum(ls);
    float* Ol = lds + w * 512 + g4 * 128 + c * 8;
    *(f4*)Ol = f4{o[0], o[1], o[2], o[3]};
    *(f4*)(Ol + 4) = f4{o[4], o[5], o[6], o[7]};
    if (l < 16 && (c & 3) == 0) { lds[4096 + (c >> 2) * 8 + w] = m; lds[4128 + (c >> 2) * 8 + w] = ls; }
    __syncthreads();
    STAMP(3);
    if (w < 2) {
      const int h = tid >> 5, d4 = tid & 31;
      const f4 m0 = *(const f4*)(lds + 4096 + h * 8), m1 = *(const f4*)(lds + 4096 + h * 8 + 4);
      const f4 l0 = *(const f4*)(lds + 4128 + h * 8), l1 = *(const f4*)(lds + 4128 + h * 8 + 4);
      const float M = fmaxf(fmaxf(fmaxf(m0[0], m0[1]), fmaxf(m0[2], m0[3])), fmaxf(fmaxf(m1[0], m1[1]), fmaxf(m1[2], m1[3])));
      f4 acc = Z4;
      float L = 0.f;
#pragma unroll
      for (int i = 0; i < 8; ++i) {
        const float wt = __builtin_amdgcn_exp2f(((i < 4 ? m0[i & 3] : m1[i & 3]) - M) * LOG2E);
        L += wt * (i < 4 ? l0[i & 3] : l1[i & 3]);
        acc += wt * *(const f4*)(lds + i * 512 + h * 128 + d4 * 4);
      }
      const float inv = __builtin_amdgcn_rcpf(L);
      const int rec = WS_B + (g * 16 + s) * REC;
      stg(rs, rec + h * 512 + d4 * 16, v4u{pk(acc[0] * inv, acc[1] * inv), tag, pk(acc[2] * inv, acc[3] * inv), tag});
      stg(rs, d4 == 0 ? rec + 2048 + h * 16 : OOB, v4u{__float_as_uint(M), tag, __float_as_uint(L), tag});
    }
    STAMP(4);
    if (xl < 4 && r < 8 && w == 2) {
      const int d = xl * 8 + r;
      const v2u xv = *(const GAS v2u*)(x + 16 * d + 4 * (l & 3));
      v4u a;
      {
        const int off = l < 32 ? WS_C + (l >> 2) * 2048 + d * 64 + (l & 3) * 16 : OOB;
        for (int spins = 0; spins < SPIN_CAP; ++spins) {
          MEMFENCE();
          a = ldg(rs, off);
          if (__all(tags_ok(a, tag) || l >= 32)) break;
        }
      }
      float hv[4] = {blo(a.x), bhi(a.x), blo(a.z), bhi(a.z)};
      const float xf[4] = {blo(xv.x), bhi(xv.x), blo(xv.y), bhi(xv.y)};
#pragma unroll
      for (int i = 0; i < 4; ++i) {
        hv[i] += dpp<ROR4>(hv[i]); hv[i] += dpp<ROR8>(hv[i]); hv[i] = xg_sum(hv[i]);
        hv[i] = rbf(xf[i] + rbf(hv[i]));
      }
      v4u y[4];
      {
        const int off = WS_D + (l >> 2) * 2048 + d * 64 + (l & 3) * 16;
        for (int spins = 0; spins < SPIN_CAP; ++spins) {
          MEMFENCE();
#pragma unroll
          for (int i = 0; i < 4; ++i) y[i] = ldg(rs, off + i * 32768);
          if (__all(tags_ok(y[0], tag) & tags_ok(y[1], tag) & tags_ok(y[2], tag) & (tags_ok(y[3], tag) || l >= 32))) break;
        }
      }
      STAMP(5);
      float v[4];
      v[0] = (blo(y[0].x) + blo(y[1].x)) + (blo(y[2].x) + blo(y[3].x));
      v[1] = (bhi(y[0].x) + bhi(y[1].x)) + (bhi(y[2].x) + bhi(y[3].x));
      v[2] = (blo(y[0].z) + blo(y[1].z)) + (blo(y[2].z) + blo(y[3].z));
      v[3] = (bhi(y[0].z) + bhi(y[1].z)) + (bhi(y[2].z) + bhi(y[3].z));
#pragma unroll
      for (int i = 0; i < 4; ++i) {
        v[i] += dpp<ROR4>(v[i]); v[i] += dpp<ROR8>(v[i]); v[i] = xg_sum(v[i]);
        v[i] = hv[i] + rbf(v[i]);
      }
      if (l < 4) *(GAS v2u*)(out + 16 * d + 4 * l) = v2u{pk(v[0], v[1]), pk(v[2], v[3])};
      if (d == 0 && l == 0) __builtin_amdgcn_raw_buffer_store_b32(epoch + 1u, rs, WS_E, 0, 16);
      STAMP(6);
    }
  } else {
    const int rr = r - 16, qi = xl * 8 + (rr >> 1), kh = rr & 1;
    const unsigned long long t0 = (DELAY_WO > 0 || DELAY_UP > 0) ? __builtin_amdgcn_s_memrealtime() : 0ull;
    const int row0 = 64 * w + 16 * g4;
    v4u Wr[8], Wt[8];
    if (w < 4) {
      const v4u xa = *(const GAS v4u*)(x + 8 * l);
      const v4u xb = *(const GAS v4u*)(x + 256 * kh + row0 + 8 * ((c >> 1) & 1));
      const bf16_t* wb = wq + (size_t)(256 * kh + row0 + 8 * (c >> 3)) * 4096 + 64 * qi + 8 * (c & 7);
#pragma unroll
      for (int j = 0; j < 8; ++j) Wr[j] = ldnt(wb + j * 4096);
      __builtin_amdgcn_sched_barrier(0);
      float xs[8] = {blo(xa.x), bhi(xa.x), blo(xa.y), bhi(xa.y), blo(xa.z), bhi(xa.z), blo(xa.w), bhi(xa.w)};
      float su = 0.f;
#pragma unroll
      for (int i = 0; i < 8; ++i) su += xs[i];
      const float mean = xg_sum(row_sum(su)) * (1.f / D);
      float sq = 0.f;
#pragma unroll
      for (int i = 0; i < 8; ++i) { const float dd = xs[i] - mean; sq += dd * dd; }
      const float var = xg_sum(row_sum(sq)) * (1.f / D);
      const float mb = rbf(mean), den = rbf(sqrtf(rbf(rbf(var) + 1e-5f)));
      v4u A;
#pragma unroll
      for (int i = 0; i < 4; ++i) A[i] = pk(rbf(blo(xb[i]) - mb) / den, rbf(bhi(xb[i]) - mb) / den);
      transpose8(Wr, Wt);
      float y[8];
#pragma unroll
      for (int e = 0; e < 8; ++e) { const f4 z = mfma(A, Wt[e], Z4); y[e] = (c & 8) ? z[2] : z[0]; }
      float* pq = lds + 3072 + (w * 2 + (c >> 3)) * 64 + (c & 7) * 8;
      if (l < 16) { *(f4*)pq = f4{y[0], y[1], y[2], y[3]}; *(f4*)(pq + 4) = f4{y[4], y[5], y[6], y[7]}; }
    }
    const unsigned epoch = __builtin_amdgcn_readfirstlane(epoch_v);
    const unsigned tag = epoch * 0x9E3779B1u + 0x7F4A7C15u;
    __syncthreads();
    STAMP(1);
    if (w == 0) {
      const float* pr = lds + 3072 + (l >> 5) * 256 + (l & 31) * 2;
      f2 acc = (*(const f2*)pr + *(const f2*)(pr + 64)) + (*(const f2*)(pr + 128) + *(const f2*)(pr + 192));
#pragma unroll
      for (int i = 0; i < 2; ++i) {
        auto t = __builtin_amdgcn_permlane32_swap(__float_as_uint(acc[i]), __float_as_uint(acc[i]), false, false);
        acc[i] = __uint_as_float(t[0]) + __uint_as_float(t[1]);
      }
      stg(rs, l < 32 ? WS_Q + xl * 8192 + kh * 4096 + (rr >> 1) * 512 + l * 16 : OOB, v4u{__float_as_uint(acc[0]), tag, __float_as_uint(acc[1]), tag});
    }
    STAMP(2);
    if (xl >= 4) {
    float* pl = lds + (w * 2 + (c >> 3)) * 64 + (c & 7) * 8;
    const int g = (xl - 4) * 2 + (rr >> 3), cb = rr & 7;
    delay_until(t0, DELAY_WO);
    {
      const bf16_t* wb = wo + (size_t)(512 * g + row0 + 8 * (c >> 3)) * D + 64 * cb + 8 * (c & 7);
#pragma unroll
      for (int j = 0; j < 8; ++j) Wr[j] = ldnt(wb + j * D);
      transpose8(Wr, Wt);
    }
    STAMP(3);
    v4u a[4], ml;
    {
      const int off = WS_B + (g * 16 + (c >> 2)) * REC + row0 * 4 + (c & 3) * 16;
      const int offm = WS_B + (g * 16 + (l >> 2)) * REC + 2048 + (w >> 1) * 16;
      for (int spins = 0; spins < SPIN_CAP; ++spins) {
        MEMFENCE();
#pragma unroll
        for (int i = 0; i < 4; ++i) a[i] = ldg(rs, off + i * 4 * REC);
        ml = ldg(rs, offm);
        if (__all(tags_ok(a[0], tag) & tags_ok(a[1], tag) & tags_ok(a[2], tag) & tags_ok(a[3], tag) & tags_ok(ml, tag))) break;
      }
    }
    STAMP(4);
    {
      const float ms = __uint_as_float(ml.x);
      float M = fmaxf(ms, dpp<ROR4>(ms)); M = fmaxf(M, dpp<ROR8>(M)); M = xg_max(M);
      const float wt = __builtin_amdgcn_exp2f((ms - M) * LOG2E) * __uint_as_float(ml.z);
      auto t = __builtin_amdgcn_permlane16_swap(__float_as_uint(wt), __float_as_uint(wt), false, false);
      auto t02 = __builtin_amdgcn_permlane32_swap(t[0], t[0], false, false);
      auto t13 = __builtin_amdgcn_permlane32_swap(t[1], t[1], false, false);
      const float w0 = __uint_as_float(t02[0]), w1_ = __uint_as_float(t13[0]), w2_ = __uint_as_float(t02[1]), w3_ = __uint_as_float(t13[1]);
      float L = (w0 + w1_) + (w2_ + w3_);
      float o[4];
      o[0] = (w0 * blo(a[0].x) + w1_ * blo(a[1].x)) + (w2_ * blo(a[2].x) + w3_ * blo(a[3].x));
      o[1] = (w0 * bhi(a[0].x) + w1_ * bhi(a[1].x)) + (w2_ * bhi(a[2].x) + w3_ * bhi(a[3].x));
      o[2] = (w0 * blo(a[0].z) + w1_ * blo(a[1].z)) + (w2_ * blo(a[2].z) + w3_ * blo(a[3].z));
      o[3] = (w0 * bhi(a[0].z) + w1_ * bhi(a[1].z)) + (w2_ * bhi(a[2].z) + w3_ * bhi(a[3].z));
      L += dpp<ROR4>(L); L += dpp<ROR8>(L);
      const float inv = __builtin_amdgcn_rcpf(L);
#pragma unroll
      for (int i = 0; i < 4; ++i) { o[i] += dpp<ROR4>(o[i]); o[i] += dpp<ROR8>(o[i]); o[i] *= inv; }
      const unsigned p01 = pk(o[0], o[1]), p23 = pk(o[2], o[3]);
      const v4u A = {p01, p23, dppu<XOR1>(p01), dppu<XOR1>(p23)};
      float y[8];
#pragma unroll
      for (int e = 0; e < 8; ++e) { const f4 z = mfma(A, Wt[e], Z4); y[e] = (c & 8) ? z[2] : z[0]; }
      if (l < 16) { *(f4*)(pl + 1024) = f4{y[0], y[1], y[2], y[3]}; *(f4*)(pl + 1028) = f4{y[4], y[5], y[6], y[7]}; }
    }
    __syncthreads();
    STAMP(5);
    if (w == 0) {
      const float* pr = lds + 1024 + g4 * 256 + c * 4;
      f4 acc = (*(const f4*)pr + *(const f4*)(pr + 64)) + (*(const f4*)(pr + 128) + *(const f4*)(pr + 192));
#pragma unroll
      for (int i = 0; i < 4; ++i) acc[i] = xg_sum(acc[i]);
      stg(rs, l < 16 ? WS_C + g * 2048 + cb * 256 + c * 16 : OOB, v4u{pk(acc[0], acc[1]), tag, pk(acc[2], acc[3]), tag});
    }
    STAMP(6);
    } else if (r < 30) {
    const int u = xl * 14 + (r - 16);
    delay_until(t0, DELAY_UP);
    v4u W2r[8], W2t[8];
    const bf16_t* wb = ((c & 4) ? w3 : w1) + (size_t)(row0 + 8 * (c >> 3)) * F + 32 * u + 8 * (c & 3);
#pragma unroll
    for (int j = 0; j < 8; ++j) Wr[j] = ldnt(wb + j * F);
    const v2u xv = *(const GAS v2u*)(x + row0 + 4 * (c & 3));
    if (w < 4) {
      const bf16_t* w2b = w2 + (size_t)(32 * u + 8 * g4) * D + 128 * w + 8 * c;
#pragma unroll
      for (int j = 0; j < 8; ++j) W2r[j] = ldnt(w2b + j * D);
    }
    __builtin_amdgcn_sched_barrier(0);
    transpose8(Wr, Wt);
    float* pl = lds + (w * 2 + (c >> 3)) * 64 + (c & 3) * 16 + ((c >> 2) & 1) * 8;
    {
      const v4u ONE = {0x3f803f80u, 0x3f803f80u, 0x3f803f80u, 0x3f803f80u};
      float y[8];
#pragma unroll
      for (int e = 0; e < 8; ++e) y[e] = mfma(ONE, Wt[e], Z4)[0];
      if (l < 16) { *(f4*)(pl + 1024) = f4{y[0], y[1], y[2], y[3]}; *(f4*)(pl + 1028) = f4{y[4], y[5], y[6], y[7]}; }
    }
    __syncthreads();
    float cs = 0.f;
    if (w < 4) {
#pragma unroll
      for (int i = 0; i < 16; ++i) cs += lds[1024 + i * 64 + l];
      transpose8(W2r, W2t);
    }
    STAMP(3);
    v4u a0, a1;
    {
      const int off = WS_C + (c >> 2) * 2048 + row0 * 4 + (c & 3) * 16;
      for (int spins = 0; spins < SPIN_CAP; ++spins) {
        MEMFENCE();
        a0 = ldg(rs, off); a1 = ldg(rs, off + 8192);
        if (__all(tags_ok(a0, tag) & tags_ok(a1, tag))) break;
      }
    }
    STAMP(4);
    float h[4] = {blo(a0.x) + blo(a1.x), bhi(a0.x) + bhi(a1.x), blo(a0.z) + blo(a1.z), bhi(a0.z) + bhi(a1.z)};
#pragma unroll
    for (int i = 0; i < 4; ++i) { h[i] += dpp<ROR4>(h[i]); h[i] += dpp<ROR8>(h[i]); }
    const unsigned h01 = pk(blo(xv.x) + rbf(h[0]), bhi(xv.x) + rbf(h[1])), h23 = pk(blo(xv.y) + rbf(h[2]), bhi(xv.y) + rbf(h[3]));
    {
      const v4u A = {h01, h23, dppu<XOR1>(h01), dppu<XOR1>(h23)};
      float y[8];
#pragma unroll
      for (int e = 0; e < 8; ++e) { const f4 z = mfma(A, Wt[e], Z4); y[e] = (c & 8) ? z[2] : z[0]; }
      h[0] = blo(h01); h[1] = bhi(h01); h[2] = blo(h23); h[3] = bhi(h23);
      float su = (h[0] + h[1]) + (h[2] + h[3]);
      su += dpp<XOR1>(su); su += dpp<XOR2>(su); su = xg_sum(su);
      const float mw = su * (1.f / 64);
      float sq = 0.f;
#pragma unroll
      for (int i = 0; i < 4; ++i) { const float dd = h[i] - mw; sq += dd * dd; }
      sq += dpp<XOR1>(sq); sq += dpp<XOR2>(sq); sq = xg_sum(sq);
      if (l < 16) { *(f4*)pl = f4{y[0], y[1], y[2], y[3]}; *(f4*)(pl + 4) = f4{y[4], y[5], y[6], y[7]}; }
      if (l == 0) { lds[2048 + w] = mw; lds[2056 + w] = sq; }
    }
    __syncthreads();
    STAMP(5);
    if (w < 4) {
      float acc = 0.f;
#pragma unroll
      for (int i = 0; i < 16; ++i) acc += lds[i * 64 + l];
      const f4 m0 = *(const f4*)(lds + 2048), m1 = *(const f4*)(lds + 2052), q0 = *(const f4*)(lds + 2056), q1 = *(const f4*)(lds + 2060);
      const float mean = (((m0[0] + m0[1]) + (m0[2] + m0[3])) + ((m1[0] + m1[1]) + (m1[2] + m1[3]))) * 0.125f;
      const float m2 = ((q0[0] + q0[1]) + (q0[2] + q0[3])) + ((q1[0] + q1[1]) + (q1[2] + q1[3]));
      float dv = 0.f;
#pragma unroll
      for (int i = 0; i < 4; ++i) { const float d0 = m0[i] - mean, d1 = m1[i] - mean; dv += d0 * d0 + d1 * d1; }
      const float var = (m2 + 64.f * dv) * (1.f / D);
      const float mb = rbf(mean), den = rbf(sqrtf(rbf(rbf(var) + 1e-5f)));
      const float val = rbf((acc - mb * cs) / den);
      const float oth = dpp<ROR8>(val);
      const float gt = (c & 8) ? oth : val, up = (c & 8) ? val : oth;
      const float si = rbf(gt * __builtin_amdgcn_rcpf(1.f + __builtin_amdgcn_exp2f(-gt * LOG2E)));
      const float hm = si * up;
      const unsigned p = pk(hm, dpp<XOR1>(hm));
      const v4u A = {p, dppu<ROR14>(p), dppu<ROR12>(p), dppu<ROR10>(p)};
      float y[8];
#pragma unroll
      for (int e = 0; e < 8; ++e) y[e] = mfma(A, W2t[e], Z4)[0];
      const unsigned s0 = l < 32 ? pk(y[0], y[1]) : pk(y[4], y[5]), s1 = l < 32 ? pk(y[2], y[3]) : pk(y[6], y[7]);
      stg(rs, (g4 & 1) ? OOB : WS_D + u * 2048 + w * 512 + c * 32 + (l >> 5) * 16, v4u{s0, tag, s1, tag});
    }
    STAMP(6);
    }
  }
  if (b == 7 && w == 2) {
    const long long tgt = 5000 + 0 * (long long)(st[0] - st[0]);
    while ((long long)(__builtin_amdgcn_s_memrealtime() - st[0]) < tgt) __builtin_amdgcn_s_sleep(4);
  }
}

extern "C" void kernel_launch(void* const* d_in, const int* in_sizes, int n_in, void* d_out, int out_size, void* d_ws, size_t ws_size,
                              hipStream_t stream) {
  fused_decode<<<256, NTHR, 0, stream>>>((const bf16_t*)d_in[0], (const bf16_t*)d_in[1], (const bf16_t*)d_in[2], (const bf16_t*)d_in[3],
                                         (const bf16_t*)d_in[4], (const bf16_t*)d_in[6], (const bf16_t*)d_in[7], (const bf16_t*)d_in[8],
                                         (bf16_t*)d_out, (unsigned char*)d_ws);
}
```

```cpp
#include <hip/hip_runtime.h>
#include <stdint.h>

typedef unsigned short bf16_t;
constexpr int NTHR = 576;
struct Srd { const bf16_t* base; uint32_t bytes, fmt; };
struct Args { Srd v, k, wq; const bf16_t* x; unsigned char* ws; const bf16_t *wo, *w1, *w3, *w2; bf16_t* out; };

__global__ void __launch_bounds__(NTHR, 3) fused_decode(Args p) {
  __shared__ __attribute__((aligned(16))) float lds[21504];
#if defined(__HIP_DEVICE_COMPILE__)
  asm volatile(
    ".Lk_code_begin:\n"
    "s_load_dwordx16 s[16:31], s[0:1], 0x0\n"
    "v_readfirstlane_b32 s3, v0\n"
    "s_and_b32 s4, s2, 7\n"
    "s_lshr_b32 s5, s2, 3\n"
    "v_and_b32 v1, 63, v0\n"
    "s_lshr_b32 s3, s3, 6\n"
    "s_sub_u32 s3, s3, 1\n"
    "s_cbranch_scc1 .Lk_auxp_19\n"
    "s_cmp_lt_u32 s5, 16\n"
    "s_cbranch_scc0 .Lk_tail_2\n"
    "s_lshl_b32 s6, s4, 8\n"
    "s_or_b32 s6, s6, s5\n"
    "s_lshl_b32 s6, s6, 3\n"
    "s_or_b32 s6, s6, s3\n"
    "s_lshl_b32 s6, s6, 13\n"
    "s_bitset1_b32 s6, 12\n"
    "v_lshlrev_b32 v3, 4, v1\n"
    "s_xor_b32 s7, s6, 0x1000\n"
    "s_waitcnt lgkmcnt(0)\n"
    "buffer_load_dwordx4 v[32:35], v3, s[20:23], s7 offen offset:0 nt\n"
    "buffer_load_dwordx4 v[36:39], v3, s[20:23], s7 offen offset:1024 nt\n"
    "buffer_load_dwordx4 v[40:43], v3, s[20:23], s7 offen offset:2048 nt\n"
    "buffer_load_dwordx4 v[44:47], v3, s[20:23], s7 offen offset:3072 nt\n"
    "buffer_load_dwordx4 v[48:51], v3, s[20:23], s6 offen offset:0 nt\n"
    "buffer_load_dwordx4 v[52:55], v3, s[20:23], s6 offen offset:1024 nt\n"
    "buffer_load_dwordx4 v[56:59], v3, s[20:23], s6 offen offset:2048 nt\n"
    "buffer_load_dwordx4 v[60:63], v3, s[20:23], s6 offen offset:3072 nt\n"
    "buffer_load_dwordx4 v[64:67], v3, s[16:19], s7 offen offset:0 nt\n"
    "buffer_load_dwordx4 v[68:71], v3, s[16:19], s7 offen offset:1024 nt\n"
    "buffer_load_dwordx4 v[72:75], v3, s[16:19], s7 offen offset:2048 nt\n"
    "buffer_load_dwordx4 v[76:79], v3, s[16:19], s7 offen offset:3072 nt\n"
    "buffer_load_dwordx4 v[80:83], v3, s[16:19], s6 offen offset:0 nt\n"
    "buffer_load_dwordx4 v[84:87], v3, s[16:19], s6 offen offset:1024 nt\n"
    "buffer_load_dwordx4 v[88:91], v3, s[16:19], s6 offen offset:2048 nt\n"
    "buffer_load_dwordx4 v[92:95], v3, s[16:19], s6 offen offset:3072 nt\n"
    "s_branch .Lk_att2_4\n"
    ".Lk_auxp_19:\n"
    "s_setprio 3\n"
    "s_mov_b32 s3, 8\n"
    "s_cmp_lt_u32 s5, 16\n"
    "s_cbranch_scc1 .Lk_attaux_5\n"
    "s_cmp_lt_u32 s5, 24\n"
    "s_cbranch_scc0 .Lk_helpa_20\n"
    "v_lshlrev_b32 v2, 4, v1\n"
    "s_waitcnt lgkmcnt(0)\n"
    "global_load_dwordx4 v[16:19], v2, s[28:29]\n"
    "s_memrealtime s[32:33]\n"
    "s_branch .Lk_qaux_6\n"
    ".Lk_helpa_20:\n"
    "s_cmp_eq_u32 s2, 249\n"
    "s_cbranch_scc0 .Lk_noto_21\n"
    "v_and_b32 v2, 31, v0\n"
    "v_lshlrev_b32 v2, 7, v2\n"
    "v_lshrrev_b32 v3, 5, v1\n"
    "v_lshl_or_b32 v2, v3, 13, v2\n"
    "s_waitcnt lgkmcnt(0)\n"
    "global_store_dword v2, v2, s[30:31] offset:2048 sc1\n"
    ".Lk_noto_21:\n"
    "s_and_b32 s6, s5, 7\n"
    "s_lshl_b32 s6, s6, 13\n"
    "s_lshl_b32 s7, s4, 10\n"
    "s_add_u32 s6, s6, s7\n"
    "v_lshlrev_b32 v2, 16, v1\n"
    "s_waitcnt lgkmcnt(0)\n"
    "s_sub_u32 s7, 0x180, s24\n"
    "s_and_b32 s7, s7, 0x380\n"
    "s_add_u32 s6, s6, s7\n"
    "buffer_load_dword v162, v2, s[24:27], s6 offen\n"
    "s_memrealtime s[32:33]\n"
    "s_branch .Lk_upaux_7\n"
    ".Lk_tail_2:\n"
    "s_sub_u32 s8, s5, 16\n"
    "s_lshl_b32 s6, s3, 19\n"
    "s_and_b32 s7, s8, 7\n"
    "s_lshl_b32 s7, s7, 10\n"
    "s_add_u32 s6, s6, s7\n"
    "s_sub_u32 s7, s4, s8\n"
    "s_add_u32 s7, s7, 3\n"
    "s_lshl_b32 s7, s7, 7\n"
    "v_lshrrev_b32 v2, 3, v1\n"
    "v_and_b32 v3, 7, v0\n"
    "v_lshlrev_b32 v2, 16, v2\n"
    "s_waitcnt lgkmcnt(0)\n"
    "s_sub_u32 s7, s7, s24\n"
    "s_and_b32 s7, s7, 0x380\n"
    "s_add_u32 s6, s6, s7\n"
    "s_cmp_lt_u32 s8, 8\n"
    "s_cbranch_scc0 .Lk_help_3\n"
    "v_lshl_or_b32 v2, v3, 4, v2\n"
    "buffer_load_dwordx4 v[32:35], v2, s[24:27], s6 offen\n"
    "s_add_u32 s6, s6, 0x2000\n"
    "buffer_load_dwordx4 v[36:39], v2, s[24:27], s6 offen\n"
    "s_add_u32 s6, s6, 0x2000\n"
    "buffer_load_dwordx4 v[40:43], v2, s[24:27], s6 offen\n"
    "s_add_u32 s6, s6, 0x2000\n"
    "buffer_load_dwordx4 v[44:47], v2, s[24:27], s6 offen\n"
    "s_add_u32 s6, s6, 0x2000\n"
    "buffer_load_dwordx4 v[48:51], v2, s[24:27], s6 offen\n"
    "s_add_u32 s6, s6, 0x2000\n"
    "buffer_load_dwordx4 v[52:55], v2, s[24:27], s6 offen\n"
    "s_add_u32 s6, s6, 0x2000\n"
    "buffer_load_dwordx4 v[56:59], v2, s[24:27], s6 offen\n"
    "s_add_u32 s6, s6, 0x2000\n"
    "buffer_load_dwordx4 v[60:63], v2, s[24:27], s6 offen\n"
    "s_branch .Lk_qw_22\n"
    ".Lk_help_3:\n"
    "s_memrealtime s[32:33]\n"
    "s_cmp_ge_u32 s3, 3\n"
    "s_cbranch_scc0 .Lk_nh_24\n"
    "v_lshl_or_b32 v2, v3, 13, v2\n"
    "buffer_load_dword v162, v2, s[24:27], s6 offen\n"
    ".Lk_nh_24:\n"
    "s_cmp_eq_u32 s2, 241\n"
    "s_cselect_b32 s9, 8, 4\n"
    "s_cmp_eq_u32 s2, 249\n"
    "s_cselect_b32 s9, 0, s9\n"
    "s_cmp_lt_u32 s3, s9\n"
    "s_cbranch_scc0 .Lk_nvh_25\n"
    "s_bitcmp0_b32 s3, 1\n"
    "s_cselect_b64 s[40:41], s[16:17], s[20:21]\n"
    "s_mov_b32 s42, 0x7fffffff\n"
    "s_mov_b32 s43, 0x20000\n"
    "s_lshl_b32 s6, s4, 24\n"
    "s_lshr_b32 s7, s3, 2\n"
    "s_add_u32 s7, s7, s8\n"
    "s_sub_u32 s7, s7, 8\n"
    "s_lshl_b32 s7, s7, 17\n"
    "s_add_u32 s6, s6, s7\n"
    "s_and_b32 s7, s3, 1\n"
    "s_lshl_b32 s7, s7, 16\n"
    "s_add_u32 s6, s6, s7\n"
    "s_add_u32 s6, s6, 57344\n"
    "v_lshlrev_b32 v2, 7, v1\n"
    ".Lk_dl_26:\n"
    "s_memrealtime s[62:63]\n"
    "s_waitcnt lgkmcnt(0)\n"
    "s_sub_u32 s62, s62, s32\n"
    "s_cmp_lt_u32 s62, 120\n"
    "s_cbranch_scc0 .Lk_dd_27\n"
    "s_sleep 1\n"
    "s_branch .Lk_dl_26\n"
    ".Lk_dd_27:\n"
    "buffer_load_dword v163, v2, s[40:43], s6 offen\n"
    "s_sub_u32 s6, s6, 0x2000\n"
    "buffer_load_dword v163, v2, s[40:43], s6 offen\n"
    ".Lk_nvh_25:\n"
    ".Lk_nohelp_23:\n"
    "s_load_dwordx8 s[92:99], s[0:1], 0x40\n"
    "s_load_dwordx2 s[34:35], s[0:1], 0x60\n"
    "s_mov_b32 s42, 0x7fffffff\n"
    "s_mov_b32 s43, 0x20000\n"
    "s_mov_b32 s36, s30\n"
    "s_and_b32 s37, s31, 0xffff\n"
    "s_mov_b32 s38, 901120\n"
    "s_mov_b32 s39, 0x20000\n"
    "s_load_dword s50, s[30:31], 0x0\n"
    "s_mov_b64 s[88:89], -1\n"
    "s_mov_b32 s52, 0x20000\n"
    "v_and_b32 v4, 15, v1\n"
    "v_lshrrev_b32 v5, 4, v1\n"
    "v_mov_b32 v9, 0xffff0000\n"
    "s_mov_b32 s44, 0xaaaaaaaa\n"
    "s_mov_b32 s45, 0xaaaaaaaa\n"
    "s_mov_b32 s46, 0xcccccccc\n"
    "s_mov_b32 s47, 0xcccccccc\n"
    "s_mov_b32 s48, 0xff00ff00\n"
    "s_mov_b32 s49, 0xff00ff00\n"
    "s_mov_b32 s14, 0x05040100\n"
    "s_mov_b32 s15, 0x07060302\n"
    "s_and_b32 s6, s4, 1\n"
    "s_lshl_b32 s6, s6, 1\n"
    "s_add_u32 s6, s6, s8\n"
    "s_cmp_lt_u32 s6, 16\n"
    "s_cbranch_scc1 .Lk_up_9\n"
    "s_branch .Lk_spare_11\n"
    ".Lk_qw_22:\n"
    "s_load_dwordx8 s[92:99], s[0:1], 0x40\n"
    "s_load_dwordx2 s[34:35], s[0:1], 0x60\n"
    "s_mov_b32 s42, 0x7fffffff\n"
    "s_mov_b32 s43, 0x20000\n"
    "s_mov_b32 s36, s30\n"
    "s_and_b32 s37, s31, 0xffff\n"
    "s_mov_b32 s38, 901120\n"
    "s_mov_b32 s39, 0x20000\n"
    "s_load_dword s50, s[30:31], 0x0\n"
    "s_mov_b64 s[88:89], -1\n"
    "s_mov_b32 s52, 0x20000\n"
    "v_and_b32 v4, 15, v1\n"
    "v_lshrrev_b32 v5, 4, v1\n"
    "v_mov_b32 v9, 0xffff0000\n"
    "s_mov_b32 s44, 0xaaaaaaaa\n"
    "s_mov_b32 s45, 0xaaaaaaaa\n"
    "s_mov_b32 s46, 0xcccccccc\n"
    "s_mov_b32 s47, 0xcccccccc\n"
    "s_mov_b32 s48, 0xff00ff00\n"
    "s_mov_b32 s49, 0xff00ff00\n"
    "s_mov_b32 s14, 0x05040100\n"
    "s_mov_b32 s15, 0x07060302\n"
    "v_mov_b32 v8, 0xffff\n"
    "v_cndmask_b32_e64 v8, v8, v9, s[44:45]\n"
    "v_cndmask_b32_e64 v6, v8, 0, s[46:47]\n"
    "v_cndmask_b32_e64 v7, 0, v8, s[46:47]\n"
    "s_lshl_b32 s6, s3, 8\n"
    "v_and_b32 v10, 4, v0\n"
    "v_lshlrev_b32 v10, 3, v10\n"
    "v_lshl_or_b32 v10, v5, 6, v10\n"
    "v_add_u32 v10, s6, v10\n"
    "v_lshrrev_b32 v11, 3, v4\n"
    "v_lshlrev_b32 v12, 5, v3\n"
    "v_lshl_or_b32 v12, v11, 8, v12\n"
    "s_lshl_b32 s7, s3, 9\n"
    "v_add_u32 v12, s7, v12\n"
    "s_waitcnt lgkmcnt(0)\n"
    "s_barrier\n"
    "ds_read_b128 v[16:19], v10 offset:16384\n"
    "ds_read_b128 v[20:23], v10 offset:16400\n"
    "s_mul_i32 s51, s50, 0x9e3779b1\n"
    "s_add_u32 s51, s51, 0x7f4a7c15\n"
    "s_and_b32 s51, s51, 0xbfffbfff\n"
    "s_waitcnt lgkmcnt(0)\n"
    "v_and_b32 v64, v16, v6\n"
    "v_and_b32 v65, v16, v7\n"
    "v_and_b32 v66, v17, v6\n"
    "v_and_b32 v67, v17, v7\n"
    "v_and_b32 v68, v18, v6\n"
    "v_and_b32 v69, v18, v7\n"
    "v_and_b32 v70, v19, v6\n"
    "v_and_b32 v71, v19, v7\n"
    "v_and_b32 v72, v20, v6\n"
    "v_and_b32 v73, v20, v7\n"
    "v_and_b32 v74, v21, v6\n"
    "v_and_b32 v75, v21, v7\n"
    "v_and_b32 v76, v22, v6\n"
    "v_and_b32 v77, v22, v7\n"
    "v_and_b32 v78, v23, v6\n"
    "v_and_b32 v79, v23, v7\n"
    "s_waitcnt vmcnt(7)\n"
    "v_mfma_f32_16x16x16_bf16 v[96:99], v[64:65], v[32:33], 0\n"
    "v_mfma_f32_16x16x16_bf16 v[100:103], v[64:65], v[34:35], 0\n"
    "s_waitcnt vmcnt(6)\n"
    "v_mfma_f32_16x16x16_bf16 v[96:99], v[66:67], v[36:37], v[96:99]\n"
    "v_mfma_f32_16x16x16_bf16 v[100:103], v[66:67], v[38:39], v[100:103]\n"
    "s_waitcnt vmcnt(5)\n"
    "v_mfma_f32_16x16x16_bf16 v[96:99], v[68:69], v[40:41], v[96:99]\n"
    "v_mfma_f32_16x16x16_bf16 v[100:103], v[68:69], v[42:43], v[100:103]\n"
    "s_waitcnt vmcnt(4)\n"
    "v_mfma_f32_16x16x16_bf16 v[96:99], v[70:71], v[44:45], v[96:99]\n"
    "v_mfma_f32_16x16x16_bf16 v[100:103], v[70:71], v[46:47], v[100:103]\n"
    "s_waitcnt vmcnt(3)\n"
    "v_mfma_f32_16x16x16_bf16 v[96:99], v[72:73], v[48:49], v[96:99]\n"
    "v_mfma_f32_16x16x16_bf16 v[100:103], v[72:73], v[50:51], v[100:103]\n"
    "s_waitcnt vmcnt(2)\n"
    "v_mfma_f32_16x16x16_bf16 v[96:99], v[74:75], v[52:53], v[96:99]\n"
    "v_mfma_f32_16x16x16_bf16 v[100:103], v[74:75], v[54:55], v[100:103]\n"
    "s_waitcnt vmcnt(1)\n"
    "v_mfma_f32_16x16x16_bf16 v[96:99], v[76:77], v[56:57], v[96:99]\n"
    "v_mfma_f32_16x16x16_bf16 v[100:103], v[76:77], v[58:59], v[100:103]\n"
    "s_waitcnt vmcnt(0)\n"
    "v_mfma_f32_16x16x16_bf16 v[96:99], v[78:79], v[60:61], v[96:99]\n"
    "v_mfma_f32_16x16x16_bf16 v[100:103], v[78:79], v[62:63], v[100:103]\n"
    "s_mov_b32 exec_lo, 0xff0000ff\n"
    "s_mov_b32 exec_hi, 0\n"
    "s_nop 4\n"
    "ds_write_b128 v12, v[96:99] offset:12288\n"
    "ds_write_b128 v12, v[100:103] offset:12304\n"
    "s_mov_b64 exec, -1\n"
    "s_waitcnt lgkmcnt(0)\n"
    "s_barrier\n"
    "s_branch .Lk_wo_8\n"
    ".Lk_qaux_6:\n"
    "s_load_dwordx8 s[92:99], s[0:1], 0x40\n"
    "s_load_dwordx2 s[34:35], s[0:1], 0x60\n"
    "s_mov_b32 s42, 0x7fffffff\n"
    "s_mov_b32 s43, 0x20000\n"
    "s_mov_b32 s36, s30\n"
    "s_and_b32 s37, s31, 0xffff\n"
    "s_mov_b32 s38, 901120\n"
    "s_mov_b32 s39, 0x20000\n"
    "s_load_dword s50, s[30:31], 0x0\n"
    "s_mov_b64 s[88:89], -1\n"
    "s_mov_b32 s52, 0x20000\n"
    "v_and_b32 v4, 15, v1\n"
    "v_lshrrev_b32 v5, 4, v1\n"
    "v_mov_b32 v9, 0xffff0000\n"
    "s_mov_b32 s44, 0xaaaaaaaa\n"
    "s_mov_b32 s45, 0xaaaaaaaa\n"
    "s_mov_b32 s46, 0xcccccccc\n"
    "s_mov_b32 s47, 0xcccccccc\n"
    "s_mov_b32 s48, 0xff00ff00\n"
    "s_mov_b32 s49, 0xff00ff00\n"
    "s_mov_b32 s14, 0x05040100\n"
    "s_mov_b32 s15, 0x07060302\n"
    "s_sub_u32 s8, s5, 16\n"
    "s_sub_u32 s13, s4, s8\n"
    "s_add_u32 s13, s13, 3\n"
    "s_lshl_b32 s13, s13, 7\n"
    "s_sub_u32 s13, s13, s24\n"
    "s_bfe_u32 s13, s13, 0x30007\n"
    "v_lshlrev_b32 v10, 5, v1\n"
    "v_lshlrev_b32 v11, 2, v1\n"
    "s_waitcnt vmcnt(0)\n"
    "v_lshlrev_b32 v20, 16, v16\n"
    "v_and_b32 v21, v9, v16\n"
    "v_lshlrev_b32 v22, 16, v17\n"
    "v_and_b32 v23, v9, v17\n"
    "v_lshlrev_b32 v24, 16, v18\n"
    "v_and_b32 v25, v9, v18\n"
    "v_lshlrev_b32 v26, 16, v19\n"
    "v_and_b32 v27, v9, v19\n"
    "v_pk_add_f32 v[28:29], v[20:21], v[22:23]\n"
    "v_pk_add_f32 v[30:31], v[24:25], v[26:27]\n"
    "v_pk_add_f32 v[28:29], v[28:29], v[30:31]\n"
    "v_add_f32 v28, v28, v29\n"
    "s_nop 1\n"
    "v_add_f32_dpp v28, v28, v28 quad_perm:[1,0,3,2] row_mask:0xf bank_mask:0xf bound_ctrl:1\n"
    "s_nop 1\n"
    "v_add_f32_dpp v28, v28, v28 quad_perm:[2,3,0,1] row_mask:0xf bank_mask:0xf bound_ctrl:1\n"
    "s_nop 1\n"
    "v_add_f32_dpp v28, v28, v28 row_ror:4 row_mask:0xf bank_mask:0xf bound_ctrl:1\n"
    "s_nop 1\n"
    "v_add_f32_dpp v28, v28, v28 row_ror:8 row_mask:0xf bank_mask:0xf bound_ctrl:1\n"
    "v_mov_b32 v29, v28\n"
    "s_nop 1\n"
    "v_permlane16_swap_b32 v28, v29\n"
    "v_add_f32 v28, v28, v29\n"
    "v_mov_b32 v29, v28\n"
    "s_nop 1\n"
    "v_permlane32_swap_b32 v28, v29\n"
    "v_add_f32 v28, v28, v29\n"
    "v_mul_f32 v28, 0x3b000000, v28\n"
    "v_sub_f32 v32, v20, v28\n"
    "v_sub_f32 v33, v21, v28\n"
    "v_sub_f32 v34, v22, v28\n"
    "v_sub_f32 v35, v23, v28\n"
    "v_sub_f32 v36, v24, v28\n"
    "v_sub_f32 v37, v25, v28\n"
    "v_sub_f32 v38, v26, v28\n"
    "v_sub_f32 v39, v27, v28\n"
    "v_mul_f32 v30, v32, v32\n"
    "v_fmac_f32 v30, v33, v33\n"
    "v_fmac_f32 v30, v34, v34\n"
    "v_fmac_f32 v30, v35, v35\n"
    "v_fmac_f32 v30, v36, v36\n"
    "v_fmac_f32 v30, v37, v37\n"
    "v_fmac_f32 v30, v38, v38\n"
    "v_fmac_f32 v30, v39, v39\n"
    "s_nop 1\n"
    "v_add_f32_dpp v30, v30, v30 quad_perm:[1,0,3,2] row_mask:0xf bank_mask:0xf bound_ctrl:1\n"
    "s_nop 1\n"
    "v_add_f32_dpp v30, v30, v30 quad_perm:[2,3,0,1] row_mask:0xf bank_mask:0xf bound_ctrl:1\n"
    "s_nop 1\n"
    "v_add_f32_dpp v30, v30, v30 row_ror:4 row_mask:0xf bank_mask:0xf bound_ctrl:1\n"
    "s_nop 1\n"
    "v_add_f32_dpp v30, v30, v30 row_ror:8 row_mask:0xf bank_mask:0xf bound_ctrl:1\n"
    "v_mov_b32 v29, v30\n"
    "s_nop 1\n"
    "v_permlane16_swap_b32 v30, v29\n"
    "v_add_f32 v30, v30, v29\n"
    "v_mov_b32 v29, v30\n"
    "s_nop 1\n"
    "v_permlane32_swap_b32 v30, v29\n"
    "v_add_f32 v30, v30, v29\n"
    "v_mul_f32 v30, 0x3b000000, v30\n"
    "v_cvt_pk_bf16_f32 v28, v28, v28\n"
    "v_and_b32 v28, v9, v28\n"
    "v_cvt_pk_bf16_f32 v30, v30, v30\n"
    "v_and_b32 v30, v9, v30\n"
    "v_add_f32 v30, 0x3727c5ac, v30\n"
    "v_cvt_pk_bf16_f32 v30, v30, v30\n"
    "v_and_b32 v30, v9, v30\n"
    "v_sqrt_f32 v30, v30\n"
    "s_nop 0\n"
    "v_cvt_pk_bf16_f32 v30, v30, v30\n"
    "v_and_b32 v30, v9, v30\n"
    "v_rcp_f32 v30, v30\n"
    "v_sub_f32 v32, v20, v28\n"
    "v_sub_f32 v33, v21, v28\n"
    "v_sub_f32 v34, v22, v28\n"
    "v_sub_f32 v35, v23, v28\n"
    "v_sub_f32 v36, v24, v28\n"
    "v_sub_f32 v37, v25, v28\n"
    "v_sub_f32 v38, v26, v28\n"
    "v_sub_f32 v39, v27, v28\n"
    "v_cvt_pk_bf16_f32 v40, v32, v33\n"
    "v_cvt_pk_bf16_f32 v41, v34, v35\n"
    "v_cvt_pk_bf16_f32 v42, v36, v37\n"
    "v_cvt_pk_bf16_f32 v43, v38, v39\n"
    "v_lshlrev_b32 v32, 16, v40\n"
    "v_and_b32 v33, v9, v40\n"
    "v_lshlrev_b32 v34, 16, v41\n"
    "v_and_b32 v35, v9, v41\n"
    "v_lshlrev_b32 v36, 16, v42\n"
    "v_and_b32 v37, v9, v42\n"
    "v_lshlrev_b32 v38, 16, v43\n"
    "v_and_b32 v39, v9, v43\n"
    "v_mul_f32 v32, v32, v30\n"
    "v_mul_f32 v33, v33, v30\n"
    "v_mul_f32 v34, v34, v30\n"
    "v_mul_f32 v35, v35, v30\n"
    "v_mul_f32 v36, v36, v30\n"
    "v_mul_f32 v37, v37, v30\n"
    "v_mul_f32 v38, v38, v30\n"
    "v_mul_f32 v39, v39, v30\n"
    "v_cvt_pk_bf16_f32 v40, v32, v32\n"
    "v_cvt_pk_bf16_f32 v41, v33, v33\n"
    "v_cvt_pk_bf16_f32 v42, v34, v34\n"
    "v_cvt_pk_bf16_f32 v43, v35, v35\n"
    "v_cvt_pk_bf16_f32 v44, v36, v36\n"
    "v_cvt_pk_bf16_f32 v45, v37, v37\n"
    "v_cvt_pk_bf16_f32 v46, v38, v38\n"
    "v_cvt_pk_bf16_f32 v47, v39, v39\n"
    "ds_write_b128 v10, v[40:43] offset:16384\n"
    "ds_write_b128 v10, v[44:47] offset:16400\n"
    "v_mov_b32 v20, 0\n"
    "ds_write_b32 v20, v20 offset:18944\n"
    "s_waitcnt lgkmcnt(0)\n"
    "s_barrier\n"
    "s_mov_b64 exec, 0\n"
    "s_mov_b64 s[88:89], 0\n"
    "s_mov_b32 s3, 0\n"
    "s_getpc_b64 s[90:91]\n"
    ".Lk_pc_29:\n"
    "s_add_u32 s90, s90, (.Lk_ret_28-.Lk_pc_29)\n"
    "s_addc_u32 s91, s91, 0\n"
    "s_branch .Lk_qrc_18\n"
    ".Lk_ret_28:\n"
    "s_mov_b64 s[88:89], -1\n"
    "s_mov_b64 exec, -1\n"
    "s_mov_b32 s3, 8\n"
    "s_nop 7\n"
    "s_mul_i32 s51, s50, 0x9e3779b1\n"
    "s_add_u32 s51, s51, 0x7f4a7c15\n"
    "s_and_b32 s51, s51, 0xbfffbfff\n"
    "s_lshr_b32 s6, s8, 2\n"
    "s_lshl_b32 s6, s6, 14\n"
    "s_and_b32 s7, s8, 3\n"
    "s_lshl_b32 s7, s7, 11\n"
    "s_add_u32 s6, s6, s7\n"
    "s_lshl_b32 s7, s13, 8\n"
    "s_add_u32 s6, s6, s7\n"
    "s_add_u32 s6, s6, 16384\n"
    "v_mov_b32 v17, s51\n"
    "s_mov_b32 s40, s92\n"
    "s_and_b32 s41, s93, 0xffff\n"
    "s_lshl_b32 s82, s4, 19\n"
    "s_lshl_b32 s83, s8, 16\n"
    "s_add_u32 s82, s82, s83\n"
    "s_sub_u32 s83, 0x180, s40\n"
    "s_and_b32 s83, s83, 0x380\n"
    "s_add_u32 s82, s82, s83\n"
    "v_lshlrev_b32 v13, 10, v1\n"
    "s_waitcnt lgkmcnt(0)\n"
    "s_barrier\n"
    ".Lk_qrc_18:\n"
    "ds_read2st64_b32 v[96:97], v11 offset0:48 offset1:49\n"
    "ds_read2st64_b32 v[98:99], v11 offset0:50 offset1:51\n"
    "ds_read2st64_b32 v[100:101], v11 offset0:52 offset1:53\n"
    "ds_read2st64_b32 v[102:103], v11 offset0:54 offset1:55\n"
    "ds_read2st64_b32 v[104:105], v11 offset0:56 offset1:57\n"
    "ds_read2st64_b32 v[106:107], v11 offset0:58 offset1:59\n"
    "ds_read2st64_b32 v[108:109], v11 offset0:60 offset1:61\n"
    "ds_read2st64_b32 v[110:111], v11 offset0:62 offset1:63\n"
    "s_waitcnt lgkmcnt(6)\n"
    "v_pk_add_f32 v[96:97], v[96:97], v[98:99]\n"
    "s_waitcnt lgkmcnt(4)\n"
    "v_pk_add_f32 v[100:101], v[100:101], v[102:103]\n"
    "s_waitcnt lgkmcnt(2)\n"
    "v_pk_add_f32 v[104:105], v[104:105], v[106:107]\n"
    "v_pk_add_f32 v[96:97], v[96:97], v[100:101]\n"
    "s_waitcnt lgkmcnt(0)\n"
    "v_pk_add_f32 v[108:109], v[108:109], v[110:111]\n"
    "v_pk_add_f32 v[104:105], v[104:105], v[108:109]\n"
    "v_pk_add_f32 v[96:97], v[96:97], v[104:105]\n"
    "v_add_f32 v18, v96, v97\n"
    "s_cbranch_execz .Lk_end_1\n"
    "s_nop 0\n"
    "v_mov_b32_dpp v19, v18 quad_perm:[1,0,3,2] row_mask:0xf bank_mask:0xf bound_ctrl:1\n"
    "s_not_b64 exec, s[44:45]\n"
    "v_cvt_pk_bf16_f32 v16, v18, v19\n"
    "buffer_store_dwordx2 v[16:17], v11, s[36:39], s6 offen sc1\n"
    "s_mov_b64 exec, -1\n"
    "buffer_load_dword v48, v13, s[40:43], s82 offen\n"
    "s_lshr_b32 s11, s4, 2\n"
    "s_lshl_b32 s11, s11, 14\n"
    "s_and_b32 s12, s4, 3\n"
    "s_lshl_b32 s12, s12, 11\n"
    "s_add_u32 s11, s11, s12\n"
    "s_add_u32 s11, s11, 49152\n"
    "v_lshrrev_b32 v10, 2, v1\n"
    "v_lshlrev_b32 v10, 7, v10\n"
    "v_and_b32 v12, 3, v0\n"
    "v_lshl_or_b32 v10, v12, 4, v10\n"
    "v_lshlrev_b32 v12, 6, v12\n"
    "v_and_b32 v13, 12, v0\n"
    "v_lshl_or_b32 v12, v13, 2, v12\n"
    "v_lshl_or_b32 v12, v5, 2, v12\n"
    "v_mov_b32 v16, 1069066811\n"
    "v_mov_b32 v20, 0\n"
    ".Lk_dl_30:\n"
    "s_memrealtime s[62:63]\n"
    "s_waitcnt lgkmcnt(0)\n"
    "s_sub_u32 s62, s62, s32\n"
    "s_cmp_lt_u32 s62, 265\n"
    "s_cbranch_scc0 .Lk_dd_31\n"
    "s_sleep 1\n"
    "s_branch .Lk_dl_30\n"
    ".Lk_dd_31:\n"
    "buffer_load_dwordx2 v[160:161], off, s[36:39], 0 offset:1024 sc1\n"
    ".Lk_dl_32:\n"
    "s_memrealtime s[62:63]\n"
    "s_waitcnt lgkmcnt(0)\n"
    "s_sub_u32 s62, s62, s32\n"
    "s_cmp_lt_u32 s62, 290\n"
    "s_cbranch_scc0 .Lk_dd_33\n"
    "s_sleep 1\n"
    "s_branch .Lk_dl_32\n"
    ".Lk_dd_33:\n"
    "s_barrier\n"
    "s_mov_b64 exec, 0\n"
    "s_mov_b64 s[88:89], 0\n"
    "s_mov_b32 s3, 0\n"
    "s_getpc_b64 s[90:91]\n"
    ".Lk_pc_35:\n"
    "s_add_u32 s90, s90, (.Lk_ret_34-.Lk_pc_35)\n"
    "s_addc_u32 s91, s91, 0\n"
    "s_branch .Lk_wtc_17\n"
    ".Lk_ret_34:\n"
    "s_mov_b64 s[88:89], -1\n"
    "s_mov_b64 exec, -1\n"
    "s_mov_b32 s3, 8\n"
    "s_nop 7\n"
    "s_mov_b32 s12, 15\n"
    "s_mov_b32 s53, 15\n"
    "s_mov_b64 exec, 0\n"
    "s_mov_b64 s[88:89], 0\n"
    "s_mov_b32 s3, 0\n"
    "s_getpc_b64 s[90:91]\n"
    ".Lk_pc_37:\n"
    "s_add_u32 s90, s90, (.Lk_ret_36-.Lk_pc_37)\n"
    "s_addc_u32 s91, s91, 0\n"
    "s_branch .Lk_woc_15\n"
    ".Lk_ret_36:\n"
    "s_mov_b64 s[88:89], -1\n"
    "s_mov_b64 exec, -1\n"
    "s_mov_b32 s3, 8\n"
    "s_nop 7\n"
    "s_branch .Lk_pchk_40\n"
    ".Lk_ppoll_38:\n"
    "buffer_load_dwordx2 v[160:161], off, s[36:39], 0 offset:1024 sc1\n"
    ".Lk_pchk_40:\n"
    "s_waitcnt vmcnt(0)\n"
    "v_readfirstlane_b32 s82, v161\n"
    "v_readfirstlane_b32 s80, v160\n"
    "s_cmp_eq_u32 s82, s51\n"
    "s_cbranch_scc1 .Lk_pdone_39\n"
    "s_sleep 2\n"
    "s_sub_u32 s52, s52, 1\n"
    "s_cmp_gt_i32 s52, 0\n"
    "s_cbranch_scc1 .Lk_ppoll_38\n"
    ".Lk_pdone_39:\n"
    "s_getreg_b32 s82, hwreg(HW_REG_XCC_ID, 2, 1)\n"
    "s_xor_b32 s80, s80, s82\n"
    "s_and_b32 s80, s80, 1\n"
    "s_lshl_b32 s80, s80, 13\n"
    ".Lk_dl_41:\n"
    "s_memrealtime s[62:63]\n"
    "s_waitcnt lgkmcnt(0)\n"
    "s_sub_u32 s62, s62, s32\n"
    "s_cmp_lt_u32 s62, 350\n"
    "s_cbranch_scc0 .Lk_dd_42\n"
    "s_sleep 1\n"
    "s_branch .Lk_dl_41\n"
    ".Lk_dd_42:\n"
    "s_add_u32 s11, s11, s80\n"
    "s_mov_b32 s53, 0\n"
    ".Lk_mlpoll_43:\n"
    "buffer_load_dwordx4 v[32:35], v10, s[36:39], s11 offen sc1\n"
    "s_waitcnt vmcnt(0)\n"
    "v_cmp_eq_u32_e64 s[54:55], s51, v33\n"
    "v_cmp_eq_u32_e64 s[58:59], s51, v35\n"
    "s_and_b64 s[54:55], s[54:55], s[58:59]\n"
    "s_and_b32 s56, s54, 0xffff\n"
    "s_cmp_eq_u32 s56, 0xffff\n"
    "s_cselect_b32 s57, 1, 0\n"
    "s_cmp_ge_u32 s54, 0xffff0000\n"
    "s_cselect_b32 s56, 2, 0\n"
    "s_or_b32 s57, s57, s56\n"
    "s_and_b32 s56, s55, 0xffff\n"
    "s_cmp_eq_u32 s56, 0xffff\n"
    "s_cselect_b32 s56, 4, 0\n"
    "s_or_b32 s57, s57, s56\n"
    "s_cmp_ge_u32 s55, 0xffff0000\n"
    "s_cselect_b32 s56, 8, 0\n"
    "s_or_b32 s57, s57, s56\n"
    "s_or_b32 s56, s57, s80\n"
    "v_mov_b32 v21, s56\n"
    "ds_write_b32 v20, v21 offset:18944\n"
    "s_cmp_eq_u32 s57, 15\n"
    "s_cbranch_scc1 .Lk_wtc_17\n"
    "s_sub_u32 s52, s52, 1\n"
    "s_cmp_gt_i32 s52, 0\n"
    "s_cbranch_scc1 .Lk_mlpoll_43\n"
    ".Lk_wtc_17:\n"
    "v_max_f32_dpp v26, v32, v32 row_ror:4 row_mask:0xf bank_mask:0xf bound_ctrl:1\n"
    "s_nop 1\n"
    "v_max_f32_dpp v26, v26, v26 row_ror:8 row_mask:0xf bank_mask:0xf bound_ctrl:1\n"
    "v_mov_b32 v27, v26\n"
    "s_nop 1\n"
    "v_permlane16_swap_b32 v26, v27\n"
    "v_max_f32 v26, v26, v27\n"
    "v_mov_b32 v27, v26\n"
    "s_nop 1\n"
    "v_permlane32_swap_b32 v26, v27\n"
    "v_max_f32 v26, v26, v27\n"
    "v_sub_f32 v26, v32, v26\n"
    "v_mul_f32 v26, v26, v16\n"
    "v_exp_f32 v26, v26\n"
    "s_nop 0\n"
    "v_mul_f32 v27, v26, v34\n"
    "s_nop 1\n"
    "v_add_f32_dpp v27, v27, v27 row_ror:4 row_mask:0xf bank_mask:0xf bound_ctrl:1\n"
    "s_nop 1\n"
    "v_add_f32_dpp v27, v27, v27 row_ror:8 row_mask:0xf bank_mask:0xf bound_ctrl:1\n"
    "v_mov_b32 v28, v27\n"
    "s_nop 1\n"
    "v_permlane16_swap_b32 v27, v28\n"
    "v_add_f32 v27, v27, v28\n"
    "v_mov_b32 v28, v27\n"
    "s_nop 1\n"
    "v_permlane32_swap_b32 v27, v28\n"
    "v_add_f32 v27, v27, v28\n"
    "v_rcp_f32 v27, v27\n"
    "s_or_b32 s57, s80, 31\n"
    "v_mov_b32 v21, s57\n"
    "v_mul_f32 v26, v26, v27\n"
    "ds_write_b32 v12, v26 offset:18432\n"
    "ds_write_b32 v20, v21 offset:18944\n"
    "s_branch .Lk_end_1\n"
    ".Lk_att2_4:\n"
    "s_load_dwordx8 s[92:99], s[0:1], 0x40\n"
    "s_load_dwordx2 s[34:35], s[0:1], 0x60\n"
    "s_mov_b32 s42, 0x7fffffff\n"
    "s_mov_b32 s43, 0x20000\n"
    "s_mov_b32 s36, s30\n"
    "s_and_b32 s37, s31, 0xffff\n"
    "s_mov_b32 s38, 901120\n"
    "s_mov_b32 s39, 0x20000\n"
    "s_load_dword s50, s[30:31], 0x0\n"
    "s_mov_b64 s[88:89], -1\n"
    "s_mov_b32 s52, 0x20000\n"
    "v_and_b32 v4, 15, v1\n"
    "v_lshrrev_b32 v5, 4, v1\n"
    "v_mov_b32 v9, 0xffff0000\n"
    "s_mov_b32 s44, 0xaaaaaaaa\n"
    "s_mov_b32 s45, 0xaaaaaaaa\n"
    "s_mov_b32 s46, 0xcccccccc\n"
    "s_mov_b32 s47, 0xcccccccc\n"
    "s_mov_b32 s48, 0xff00ff00\n"
    "s_mov_b32 s49, 0xff00ff00\n"
    "s_mov_b32 s14, 0x05040100\n"
    "s_mov_b32 s15, 0x07060302\n"
    "s_lshl_b32 s8, s3, 2\n"
    "v_lshrrev_b32 v10, 2, v4\n"
    "v_lshlrev_b32 v10, 5, v10\n"
    "v_add_u32 v10, s8, v10\n"
    "v_lshlrev_b32 v11, 5, v5\n"
    "v_add_u32 v12, s8, v11\n"
    "v_and_b32 v13, 7, v0\n"
    "v_lshl_or_b32 v11, v13, 2, v11\n"
    "s_lshl_b32 s9, s3, 8\n"
    "v_lshlrev_b32 v13, 2, v1\n"
    "v_add_u32 v13, s9, v13\n"
    "s_lshl_b32 s10, s3, 11\n"
    "v_lshlrev_b32 v14, 5, v1\n"
    "v_add_u32 v14, s10, v14\n"
    "s_lshl_b32 s8, s4, 2\n"
    "s_lshr_b32 s9, s5, 2\n"
    "s_add_u32 s8, s8, s9\n"
    "s_lshl_b32 s8, s8, 14\n"
    "s_and_b32 s9, s5, 3\n"
    "s_lshl_b32 s9, s9, 11\n"
    "s_add_u32 s8, s8, s9\n"
    "s_add_u32 s8, s8, 114688\n"
    "s_lshl_b32 s9, s3, 9\n"
    "v_and_b32 v17, 31, v0\n"
    "v_lshlrev_b32 v17, 4, v17\n"
    "v_add_u32 v17, s9, v17\n"
    "v_lshrrev_b32 v16, 5, v1\n"
    "v_lshl_or_b32 v15, v16, 13, v17\n"
    "v_lshl_or_b32 v17, v16, 3, v17\n"
    "v_mov_b32 v16, 1069066811\n"
    "v_mov_b32 v18, 1035273459\n"
    "s_waitcnt lgkmcnt(0)\n"
    "s_mul_i32 s51, s50, 0x9e3779b1\n"
    "s_add_u32 s51, s51, 0x7f4a7c15\n"
    "s_and_b32 s51, s51, 0xbfffbfff\n"
    "v_mov_b32 v29, s51\n"
    "v_mov_b32 v31, s51\n"
    "s_lshl_b32 s9, s3, 13\n"
    "s_add_u32 s9, s9, 20480\n"
    "v_xor_b32 v20, v4, v5\n"
    "v_lshlrev_b32 v20, 4, v20\n"
    "v_lshl_or_b32 v20, v5, 8, v20\n"
    "v_add_u32 v20, s9, v20\n"
    "v_xor_b32 v21, 64, v20\n"
    "v_xor_b32 v22, 0x80, v20\n"
    "v_xor_b32 v23, 0xc0, v20\n"
    "v_and_b32 v24, 3, v0\n"
    "v_lshrrev_b32 v25, 2, v4\n"
    "v_lshl_or_b32 v24, v24, 2, v25\n"
    "v_xor_b32 v26, v24, v5\n"
    "v_lshlrev_b32 v26, 4, v26\n"
    "v_lshl_or_b32 v24, v24, 8, v26\n"
    "v_add_u32 v24, s9, v24\n"
    "v_lshlrev_b32 v2, 4, v5\n"
    "v_lshl_or_b32 v2, v25, 8, v2\n"
    "v_xor_b32 v25, 64, v24\n"
    "v_xor_b32 v26, 0x80, v24\n"
    "v_xor_b32 v27, 0xc0, v24\n"
    "s_waitcnt vmcnt(15)\n"
    "ds_write_b128 v20, v[32:35] offset:0\n"
    "s_waitcnt vmcnt(14)\n"
    "ds_write_b128 v21, v[36:39] offset:1024\n"
    "s_waitcnt vmcnt(13)\n"
    "ds_write_b128 v22, v[40:43] offset:2048\n"
    "s_waitcnt vmcnt(12)\n"
    "ds_write_b128 v23, v[44:47] offset:3072\n"
    "s_waitcnt vmcnt(11)\n"
    "ds_write_b128 v20, v[48:51] offset:4096\n"
    "s_waitcnt vmcnt(10)\n"
    "ds_write_b128 v21, v[52:55] offset:5120\n"
    "s_waitcnt vmcnt(9)\n"
    "ds_write_b128 v22, v[56:59] offset:6144\n"
    "s_waitcnt vmcnt(8)\n"
    "ds_write_b128 v23, v[60:63] offset:7168\n"
    "s_waitcnt lgkmcnt(0)\n"
    "ds_read_b128 v[32:35], v24 offset:0\n"
    "ds_read_b128 v[36:39], v25 offset:0\n"
    "ds_read_b128 v[40:43], v26 offset:0\n"
    "ds_read_b128 v[44:47], v27 offset:0\n"
    "ds_read_b128 v[48:51], v24 offset:4096\n"
    "ds_read_b128 v[52:55], v25 offset:4096\n"
    "ds_read_b128 v[56:59], v26 offset:4096\n"
    "ds_read_b128 v[60:63], v27 offset:4096\n"
    "s_waitcnt vmcnt(6)\n"
    "v_perm_b32 v128, v68, v64, s14\n"
    "v_perm_b32 v132, v68, v64, s15\n"
    "v_perm_b32 v136, v69, v65, s14\n"
    "v_perm_b32 v140, v69, v65, s15\n"
    "v_perm_b32 v144, v70, v66, s14\n"
    "v_perm_b32 v148, v70, v66, s15\n"
    "v_perm_b32 v152, v71, v67, s14\n"
    "v_perm_b32 v156, v71, v67, s15\n"
    "s_waitcnt vmcnt(4)\n"
    "v_perm_b32 v129, v76, v72, s14\n"
    "v_perm_b32 v133, v76, v72, s15\n"
    "v_perm_b32 v137, v77, v73, s14\n"
    "v_perm_b32 v141, v77, v73, s15\n"
    "v_perm_b32 v145, v78, v74, s14\n"
    "v_perm_b32 v149, v78, v74, s15\n"
    "v_perm_b32 v153, v79, v75, s14\n"
    "v_perm_b32 v157, v79, v75, s15\n"
    "s_waitcnt lgkmcnt(0)\n"
    "s_barrier\n"
    ".Lk_attc_12:\n"
    "ds_read_b128 v[64:67], v2 offset:18688\n"
    "ds_read_b128 v[68:71], v2 offset:18752\n"
    "ds_read_b128 v[72:75], v2 offset:18816\n"
    "ds_read_b128 v[76:79], v2 offset:18880\n"
    "s_waitcnt lgkmcnt(3)\n"
    "v_mfma_f32_16x16x32_bf16 v[96:99], v[32:35], v[64:67], 0\n"
    "v_mfma_f32_16x16x32_bf16 v[100:103], v[48:51], v[64:67], 0\n"
    "s_waitcnt lgkmcnt(2)\n"
    "v_mfma_f32_16x16x32_bf16 v[96:99], v[36:39], v[68:71], v[96:99]\n"
    "v_mfma_f32_16x16x32_bf16 v[100:103], v[52:55], v[68:71], v[100:103]\n"
    "s_waitcnt lgkmcnt(1)\n"
    "v_mfma_f32_16x16x32_bf16 v[96:99], v[40:43], v[72:75], v[96:99]\n"
    "v_mfma_f32_16x16x32_bf16 v[100:103], v[56:59], v[72:75], v[100:103]\n"
    "s_waitcnt lgkmcnt(0)\n"
    "v_mfma_f32_16x16x32_bf16 v[96:99], v[44:47], v[76:79], v[96:99]\n"
    "v_mfma_f32_16x16x32_bf16 v[100:103], v[60:63], v[76:79], v[100:103]\n"
    "s_nop 6\n"
    "v_cndmask_b32_e64 v40, v96, v98, s[44:45]\n"
    "v_cndmask_b32_e64 v41, v97, v99, s[44:45]\n"
    "v_cndmask_b32_e64 v42, v100, v102, s[44:45]\n"
    "v_cndmask_b32_e64 v43, v101, v103, s[44:45]\n"
    "v_cndmask_b32_e64 v40, v40, v42, s[46:47]\n"
    "v_cndmask_b32_e64 v41, v41, v43, s[46:47]\n"
    "v_cvt_pk_bf16_f32 v42, v40, v41\n"
    "v_lshlrev_b32 v40, 16, v42\n"
    "v_and_b32 v41, v9, v42\n"
    "v_mul_f32 v40, v40, v18\n"
    "v_mul_f32 v41, v41, v18\n"
    "v_cvt_pk_bf16_f32 v42, v40, v41\n"
    "v_lshlrev_b32 v40, 16, v42\n"
    "v_and_b32 v41, v9, v42\n"
    "v_max_f32 v44, v40, v41\n"
    "s_nop 1\n"
    "v_max_f32_dpp v44, v44, v44 quad_perm:[1,0,3,2] row_mask:0xf bank_mask:0xf bound_ctrl:1\n"
    "s_nop 1\n"
    "v_max_f32_dpp v44, v44, v44 quad_perm:[2,3,0,1] row_mask:0xf bank_mask:0xf bound_ctrl:1\n"
    "v_mov_b32 v45, v44\n"
    "s_nop 1\n"
    "v_permlane16_swap_b32 v44, v45\n"
    "v_max_f32 v44, v44, v45\n"
    "v_mov_b32 v45, v44\n"
    "s_nop 1\n"
    "v_permlane32_swap_b32 v44, v45\n"
    "v_max_f32 v44, v44, v45\n"
    "ds_write_b32 v10, v44 offset:16384\n"
    "v_sub_f32 v40, v40, v44\n"
    "v_sub_f32 v41, v41, v44\n"
    "v_mul_f32 v40, v40, v16\n"
    "v_mul_f32 v41, v41, v16\n"
    "v_exp_f32 v40, v40\n"
    "v_exp_f32 v41, v41\n"
    "s_nop 0\n"
    "v_add_f32 v44, v40, v41\n"
    "v_cvt_pk_bf16_f32 v42, v40, v41\n"
    "ds_write_b32 v13, v44 offset:16640\n"
    "s_nop 0\n"
    "v_mov_b32_dpp v48, v42 quad_perm:[0,0,0,0] row_mask:0xf bank_mask:0xf bound_ctrl:1\n"
    "v_mov_b32_dpp v49, v42 quad_perm:[1,1,1,1] row_mask:0xf bank_mask:0xf bound_ctrl:1\n"
    "v_mov_b32_dpp v50, v42 quad_perm:[2,2,2,2] row_mask:0xf bank_mask:0xf bound_ctrl:1\n"
    "v_mov_b32_dpp v51, v42 quad_perm:[3,3,3,3] row_mask:0xf bank_mask:0xf bound_ctrl:1\n"
    "s_waitcnt vmcnt(2)\n"
    "v_perm_b32 v130, v84, v80, s14\n"
    "v_perm_b32 v134, v84, v80, s15\n"
    "v_perm_b32 v138, v85, v81, s14\n"
    "v_perm_b32 v142, v85, v81, s15\n"
    "v_perm_b32 v146, v86, v82, s14\n"
    "v_perm_b32 v150, v86, v82, s15\n"
    "v_perm_b32 v154, v87, v83, s14\n"
    "v_perm_b32 v158, v87, v83, s15\n"
    "s_waitcnt vmcnt(0)\n"
    "v_perm_b32 v131, v92, v88, s14\n"
    "v_perm_b32 v135, v92, v88, s15\n"
    "v_perm_b32 v139, v93, v89, s14\n"
    "v_perm_b32 v143, v93, v89, s15\n"
    "v_perm_b32 v147, v94, v90, s14\n"
    "v_perm_b32 v151, v94, v90, s15\n"
    "v_perm_b32 v155, v95, v91, s14\n"
    "v_perm_b32 v159, v95, v91, s15\n"
    "v_mfma_f32_16x16x32_bf16 v[96:99], v[48:51], v[128:131], 0\n"
    "v_mfma_f32_16x16x32_bf16 v[100:103], v[48:51], v[132:135], 0\n"
    "s_waitcnt lgkmcnt(0)\n"
    "s_cbranch_execz .Lk_nb_45\n"
    "s_barrier\n"
    ".Lk_nb_45:\n"
    "ds_read_b32 v24, v11 offset:16384\n"
    "ds_read_b32 v25, v12 offset:16384\n"
    "v_mfma_f32_16x16x32_bf16 v[104:107], v[48:51], v[136:139], 0\n"
    "v_mfma_f32_16x16x32_bf16 v[108:111], v[48:51], v[140:143], 0\n"
    "v_mfma_f32_16x16x32_bf16 v[112:115], v[48:51], v[144:147], 0\n"
    "v_mfma_f32_16x16x32_bf16 v[116:119], v[48:51], v[148:151], 0\n"
    "v_mfma_f32_16x16x32_bf16 v[120:123], v[48:51], v[152:155], 0\n"
    "v_mfma_f32_16x16x32_bf16 v[124:127], v[48:51], v[156:159], 0\n"
    "s_waitcnt lgkmcnt(0)\n"
    "v_max_f32_dpp v24, v24, v24 quad_perm:[1,0,3,2] row_mask:0xf bank_mask:0xf bound_ctrl:1\n"
    "s_nop 1\n"
    "v_max_f32_dpp v24, v24, v24 quad_perm:[2,3,0,1] row_mask:0xf bank_mask:0xf bound_ctrl:1\n"
    "s_nop 1\n"
    "v_max_f32_dpp v24, v24, v24 row_half_mirror row_mask:0xf bank_mask:0xf bound_ctrl:1\n"
    "v_sub_f32 v25, v25, v24\n"
    "v_mul_f32 v25, v25, v16\n"
    "v_exp_f32 v24, v25\n"
    "s_cmp_lt_u32 s3, 4\n"
    "v_mul_f32 v52, v96, v24\n"
    "v_mul_f32 v53, v100, v24\n"
    "v_mul_f32 v54, v104, v24\n"
    "v_mul_f32 v55, v108, v24\n"
    "ds_write_b128 v14, v[52:55]\n"
    "v_mul_f32 v56, v112, v24\n"
    "v_mul_f32 v57, v116, v24\n"
    "v_mul_f32 v58, v120, v24\n"
    "v_mul_f32 v59, v124, v24\n"
    "ds_write_b128 v14, v[56:59] offset:16\n"
    "s_waitcnt lgkmcnt(0)\n"
    "s_cbranch_execz .Lk_nb_46\n"
    "s_barrier\n"
    ".Lk_nb_46:\n"
    "s_cbranch_scc0 .Lk_final_10\n"
    "ds_read2st64_b64 v[32:35], v17 offset0:0 offset1:4\n"
    "ds_read2st64_b64 v[36:39], v17 offset0:8 offset1:12\n"
    "ds_read2st64_b64 v[40:43], v17 offset0:16 offset1:20\n"
    "ds_read2st64_b64 v[44:47], v17 offset0:24 offset1:28\n"
    "s_waitcnt lgkmcnt(3)\n"
    "v_pk_add_f32 v[32:33], v[32:33], v[34:35]\n"
    "s_waitcnt lgkmcnt(2)\n"
    "v_pk_add_f32 v[36:37], v[36:37], v[38:39]\n"
    "s_waitcnt lgkmcnt(1)\n"
    "v_pk_add_f32 v[40:41], v[40:41], v[42:43]\n"
    "v_pk_add_f32 v[32:33], v[32:33], v[36:37]\n"
    "s_waitcnt lgkmcnt(0)\n"
    "v_pk_add_f32 v[44:45], v[44:45], v[46:47]\n"
    "v_pk_add_f32 v[40:41], v[40:41], v[44:45]\n"
    "v_pk_add_f32 v[32:33], v[32:33], v[40:41]\n"
    "v_cvt_pk_bf16_f32 v28, v32, v33\n"
    "v_cvt_pk_bf16_f32 v30, v32, v33\n"
    "s_cbranch_execz .Lk_end_1\n"
    "s_nop 0\n"
    "v_permlane32_swap_b32 v28, v30\n"
    "buffer_store_dwordx4 v[28:31], v15, s[36:39], s8 offen sc1\n"
    "s_branch .Lk_end_1\n"
    ".Lk_attaux_5:\n"
    "s_waitcnt lgkmcnt(0)\n"
    "s_memrealtime s[32:33]\n"
    "s_getpc_b64 s[12:13]\n"
    ".Lk_pc:\n"
    "s_sub_u32 s12, s12, (.Lk_pc-.Lk_code_begin)\n"
    "s_subb_u32 s13, s13, 0\n"
    "s_lshl_b32 s10, s5, 7\n"
    "s_min_u32 s11, s10, (.Lk_code_end-.Lk_code_begin-4)\n"
    "s_load_dword s56, s[12:13], s11\n"
    "s_add_u32 s10, s10, 0x800\n"
    "s_min_u32 s11, s10, (.Lk_code_end-.Lk_code_begin-4)\n"
    "s_load_dword s56, s[12:13], s11\n"
    "s_add_u32 s10, s10, 0x800\n"
    "s_min_u32 s11, s10, (.Lk_code_end-.Lk_code_begin-4)\n"
    "s_load_dword s56, s[12:13], s11\n"
    "s_add_u32 s10, s10, 0x800\n"
    "s_min_u32 s11, s10, (.Lk_code_end-.Lk_code_begin-4)\n"
    "s_load_dword s56, s[12:13], s11\n"
    "s_add_u32 s10, s10, 0x800\n"
    "s_min_u32 s11, s10, (.Lk_code_end-.Lk_code_begin-4)\n"
    "s_load_dword s56, s[12:13], s11\n"
    "s_add_u32 s10, s10, 0x800\n"
    "s_min_u32 s11, s10, (.Lk_code_end-.Lk_code_begin-4)\n"
    "s_load_dword s56, s[12:13], s11\n"
    "s_add_u32 s10, s10, 0x800\n"
    "s_min_u32 s11, s10, (.Lk_code_end-.Lk_code_begin-4)\n"
    "s_load_dword s56, s[12:13], s11\n"
    "s_add_u32 s10, s10, 0x800\n"
    "s_waitcnt lgkmcnt(0)\n"
    "s_load_dwordx8 s[92:99], s[0:1], 0x40\n"
    "s_load_dwordx2 s[34:35], s[0:1], 0x60\n"
    "s_mov_b32 s42, 0x7fffffff\n"
    "s_mov_b32 s43, 0x20000\n"
    "s_mov_b32 s36, s30\n"
    "s_and_b32 s37, s31, 0xffff\n"
    "s_mov_b32 s38, 901120\n"
    "s_mov_b32 s39, 0x20000\n"
    "s_load_dword s50, s[30:31], 0x0\n"
    "s_mov_b64 s[88:89], -1\n"
    "s_mov_b32 s52, 0x20000\n"
    "v_and_b32 v4, 15, v1\n"
    "v_lshrrev_b32 v5, 4, v1\n"
    "v_mov_b32 v9, 0xffff0000\n"
    "s_mov_b32 s44, 0xaaaaaaaa\n"
    "s_mov_b32 s45, 0xaaaaaaaa\n"
    "s_mov_b32 s46, 0xcccccccc\n"
    "s_mov_b32 s47, 0xcccccccc\n"
    "s_mov_b32 s48, 0xff00ff00\n"
    "s_mov_b32 s49, 0xff00ff00\n"
    "s_mov_b32 s14, 0x05040100\n"
    "s_mov_b32 s15, 0x07060302\n"
    "s_mov_b64 exec, 0\n"
    "s_mov_b64 s[88:89], 0\n"
    "s_mov_b32 s3, 0\n"
    "s_getpc_b64 s[90:91]\n"
    ".Lk_pc_48:\n"
    "s_add_u32 s90, s90, (.Lk_ret_47-.Lk_pc_48)\n"
    "s_addc_u32 s91, s91, 0\n"
    "s_branch .Lk_attc_12\n"
    ".Lk_ret_47:\n"
    "s_mov_b64 s[88:89], -1\n"
    "s_mov_b64 exec, -1\n"
    "s_mov_b32 s3, 8\n"
    "s_nop 7\n"
    "s_mov_b64 exec, 0\n"
    "s_mov_b64 s[88:89], 0\n"
    "s_mov_b32 s3, 0\n"
    "s_getpc_b64 s[90:91]\n"
    ".Lk_pc_50:\n"
    "s_add_u32 s90, s90, (.Lk_ret_49-.Lk_pc_50)\n"
    "s_addc_u32 s91, s91, 0\n"
    "s_branch .Lk_mlc_14\n"
    ".Lk_ret_49:\n"
    "s_mov_b64 s[88:89], -1\n"
    "s_mov_b64 exec, -1\n"
    "s_mov_b32 s3, 8\n"
    "s_nop 7\n"
    "s_cmp_lt_u32 s5, 4\n"
    "s_cbranch_scc0 .Lk_nofin_51\n"
    "s_mov_b64 exec, 0\n"
    "s_mov_b64 s[88:89], 0\n"
    "s_mov_b32 s3, 0\n"
    "s_getpc_b64 s[90:91]\n"
    ".Lk_pc_53:\n"
    "s_add_u32 s90, s90, (.Lk_ret_52-.Lk_pc_53)\n"
    "s_addc_u32 s91, s91, 0\n"
    "s_branch .Lk_finc_13\n"
    ".Lk_ret_52:\n"
    "s_mov_b64 s[88:89], -1\n"
    "s_mov_b64 exec, -1\n"
    "s_mov_b32 s3, 8\n"
    "s_nop 7\n"
    ".Lk_nofin_51:\n"
    "v_lshlrev_b32 v2, 4, v1\n"
    "v_lshlrev_b32 v3, 3, v1\n"
    "v_add_u32 v3, 18688, v3\n"
    "s_lshr_b32 s8, s4, 2\n"
    "s_lshl_b32 s8, s8, 14\n"
    "s_and_b32 s9, s4, 3\n"
    "s_lshl_b32 s9, s9, 11\n"
    "s_add_u32 s8, s8, s9\n"
    "s_add_u32 s6, s8, 16384\n"
    "s_lshl_b32 s9, s5, 7\n"
    "s_add_u32 s8, s8, s9\n"
    "s_add_u32 s8, s8, 49152\n"
    "v_and_b32 v16, 1, v0\n"
    "v_lshlrev_b32 v15, 4, v5\n"
    "v_and_b32 v11, 7, v0\n"
    "v_lshl_or_b32 v12, v11, 8, v15\n"
    "v_lshrrev_b32 v13, 3, v4\n"
    "v_lshl_or_b32 v12, v13, 7, v12\n"
    "v_lshl_or_b32 v15, v16, 13, v15\n"
    "v_lshlrev_b32 v10, 5, v5\n"
    "v_lshl_or_b32 v10, v11, 2, v10\n"
    "v_mov_b32 v16, 1069066811\n"
    "s_mov_b32 s10, 0x00030003\n"
    "s_mov_b32 s11, 0x00030003\n"
    "s_waitcnt lgkmcnt(0)\n"
    "s_mul_i32 s51, s50, 0x9e3779b1\n"
    "s_add_u32 s51, s51, 0x7f4a7c15\n"
    "s_and_b32 s51, s51, 0xbfffbfff\n"
    "v_mov_b32 v29, s51\n"
    "v_mov_b32 v31, s51\n"
    ".Lk_dl_54:\n"
    "s_memrealtime s[62:63]\n"
    "s_waitcnt lgkmcnt(0)\n"
    "s_sub_u32 s62, s62, s32\n"
    "s_cmp_lt_u32 s62, 300\n"
    "s_cbranch_scc0 .Lk_dd_55\n"
    "s_sleep 1\n"
    "s_branch .Lk_dl_54\n"
    ".Lk_dd_55:\n"
    ".Lk_qpoll_56:\n"
    "buffer_load_dwordx4 v[32:35], v2, s[36:39], s6 offen sc1\n"
    "buffer_load_dwordx4 v[36:39], v2, s[36:39], s6 offen offset:1024 sc1\n"
    "s_waitcnt vmcnt(0)\n"
    "ds_write2_b32 v3, v32, v34 offset0:0 offset1:1\n"
    "ds_write2_b32 v3, v36, v38 offset0:128 offset1:129\n"
    "v_cmp_eq_u32_e64 s[54:55], s51, v33\n"
    "v_cmp_eq_u32_e64 s[58:59], s51, v35\n"
    "s_and_b64 s[54:55], s[54:55], s[58:59]\n"
    "v_cmp_eq_u32_e64 s[56:57], s51, v37\n"
    "v_cmp_eq_u32_e64 s[58:59], s51, v39\n"
    "s_and_b64 s[56:57], s[56:57], s[58:59]\n"
    "s_and_b64 s[54:55], s[54:55], s[56:57]\n"
    "s_cmp_eq_u64 s[54:55], -1\n"
    "s_cbranch_scc1 .Lk_qdone_57\n"
    "s_sub_u32 s52, s52, 1\n"
    "s_cmp_gt_i32 s52, 0\n"
    "s_cbranch_scc1 .Lk_qpoll_56\n"
    ".Lk_qdone_57:\n"
    "s_waitcnt lgkmcnt(0)\n"
    "s_barrier\n"
    "s_barrier\n"
    ".Lk_mlc_14:\n"
    "ds_read_b32 v32, v10 offset:16384\n"
    "ds_read_b128 v[36:39], v12 offset:16640\n"
    "ds_read_b128 v[40:43], v12 offset:16704\n"
    "s_waitcnt lgkmcnt(2)\n"
    "v_max_f32_dpp v28, v32, v32 quad_perm:[1,0,3,2] row_mask:0xf bank_mask:0xf bound_ctrl:1\n"
    "s_waitcnt lgkmcnt(0)\n"
    "v_pk_add_f32 v[36:37], v[36:37], v[38:39]\n"
    "v_max_f32_dpp v28, v28, v28 quad_perm:[2,3,0,1] row_mask:0xf bank_mask:0xf bound_ctrl:1\n"
    "v_pk_add_f32 v[40:41], v[40:41], v[42:43]\n"
    "v_pk_add_f32 v[36:37], v[36:37], v[40:41]\n"
    "v_max_f32_dpp v28, v28, v28 row_half_mirror row_mask:0xf bank_mask:0xf bound_ctrl:1\n"
    "v_add_f32 v36, v36, v37\n"
    "v_sub_f32 v32, v32, v28\n"
    "v_mul_f32 v32, v32, v16\n"
    "v_exp_f32 v32, v32\n"
    "s_nop 0\n"
    "v_mul_f32 v30, v32, v36\n"
    "s_nop 1\n"
    "v_add_f32_dpp v30, v30, v30 quad_perm:[1,0,3,2] row_mask:0xf bank_mask:0xf bound_ctrl:1\n"
    "s_nop 1\n"
    "v_add_f32_dpp v30, v30, v30 quad_perm:[2,3,0,1] row_mask:0xf bank_mask:0xf bound_ctrl:1\n"
    "s_nop 1\n"
    "v_add_f32_dpp v30, v30, v30 row_ror:4 row_mask:0xf bank_mask:0xf bound_ctrl:1\n"
    "s_nop 1\n"
    "v_add_f32_dpp v30, v30, v30 row_ror:8 row_mask:0xf bank_mask:0xf bound_ctrl:1\n"
    "s_cbranch_execz .Lk_end_1\n"
    "s_mov_b64 exec, s[10:11]\n"
    "buffer_store_dwordx4 v[28:31], v15, s[36:39], s8 offen sc1\n"
    "s_mov_b64 exec, -1\n"
    "s_barrier\n"
    "s_branch .Lk_end_1\n"
    ".Lk_final_10:\n"
    "s_cmp_lt_u32 s3, 6\n"
    "s_cselect_b64 s[40:41], s[94:95], s[96:97]\n"
    "s_and_b32 s41, s41, 0xffff\n"
    "s_mov_b32 s42, 1835008\n"
    "s_mul_i32 s8, s4, 7\n"
    "s_and_b32 s9, s4, 1\n"
    "s_add_u32 s8, s8, s9\n"
    "s_lshl_b32 s8, s8, 6\n"
    "s_lshl_b32 s9, s5, 1\n"
    "s_and_b32 s10, s3, 1\n"
    "s_add_u32 s9, s9, s10\n"
    "s_mul_i32 s9, s9, 57344\n"
    "s_add_u32 s8, s8, s9\n"
    "v_lshrrev_b32 v10, 2, v1\n"
    "v_mul_u32_u24 v10, 3584, v10\n"
    "v_and_b32 v11, 3, v0\n"
    "v_lshl_add_u32 v10, v11, 7, v10\n"
    "s_sleep 10\n"
    "buffer_load_dword v162, v10, s[40:43], s8 offen\n"
    "s_cmp_eq_u32 s3, 7\n"
    "s_cbranch_scc0 .Lk_end_1\n"
    "s_cmp_lt_u32 s5, 4\n"
    "s_cbranch_scc0 .Lk_end_1\n"
    "s_sleep 4\n"
    "s_lshl_b32 s8, s4, 2\n"
    "s_add_u32 s8, s8, s5\n"
    "s_lshl_b32 s9, s8, 5\n"
    "v_and_b32 v10, 3, v0\n"
    "v_lshlrev_b32 v11, 3, v10\n"
    "v_add_u32 v11, s9, v11\n"
    "global_load_dwordx2 v[20:21], v11, s[28:29]\n"
    ".Lk_ppoll_58:\n"
    "buffer_load_dwordx2 v[160:161], off, s[36:39], 0 offset:1152 sc1\n"
    ".Lk_pchk_60:\n"
    "s_waitcnt vmcnt(0)\n"
    "v_readfirstlane_b32 s82, v161\n"
    "v_readfirstlane_b32 s80, v160\n"
    "s_cmp_eq_u32 s82, s51\n"
    "s_cbranch_scc1 .Lk_pdone_59\n"
    "s_sleep 2\n"
    "s_sub_u32 s52, s52, 1\n"
    "s_cmp_gt_i32 s52, 0\n"
    "s_cbranch_scc1 .Lk_ppoll_58\n"
    ".Lk_pdone_59:\n"
    "s_getreg_b32 s82, hwreg(HW_REG_XCC_ID, 2, 1)\n"
    "s_xor_b32 s80, s80, s82\n"
    "s_and_b32 s80, s80, 1\n"
    "s_lshl_b32 s80, s80, 13\n"
    "v_lshlrev_b32 v12, 4, v10\n"
    "v_bfe_u32 v13, v0, 2, 2\n"
    "v_lshl_or_b32 v12, v13, 11, v12\n"
    "v_and_b32 v13, 1, v5\n"
    "v_lshl_or_b32 v14, v13, 14, v12\n"
    "v_lshl_or_b32 v12, v5, 14, v12\n"
    "s_lshl_b32 s9, s8, 6\n"
    "s_add_u32 s9, s9, s80\n"
    "s_add_u32 s6, s9, 638976\n"
    "s_add_u32 s10, s9, 671744\n"
    "s_add_u32 s11, s10, 65536\n"
    "s_add_u32 s12, s10, 131072\n"
    "s_add_u32 s13, s10, 196608\n"
    "v_mov_b32 v8, 0xffff\n"
    "v_cndmask_b32_e64 v8, v8, v9, s[44:45]\n"
    "v_cndmask_b32_e64 v6, v8, 0, s[46:47]\n"
    "v_cndmask_b32_e64 v7, 0, v8, s[46:47]\n"
    "v_and_b32 v24, 0x3f803f80, v6\n"
    "v_mov_b32 v25, 0\n"
    "v_and_b32 v26, 0x3f803f80, v7\n"
    "v_mov_b32 v27, 0\n"
    "buffer_load_dwordx4 v[64:67], v12, s[36:39], s10 offen sc1\n"
    "buffer_load_dwordx4 v[68:71], v12, s[36:39], s11 offen sc1\n"
    "buffer_load_dwordx4 v[72:75], v12, s[36:39], s12 offen sc1\n"
    "buffer_load_dwordx4 v[76:79], v12, s[36:39], s13 offen sc1\n"
    ".Lk_hpoll_61:\n"
    "buffer_load_dwordx4 v[32:35], v14, s[36:39], s6 offen sc1\n"
    "s_waitcnt vmcnt(0)\n"
    "v_cmp_eq_u32_e64 s[54:55], s51, v33\n"
    "v_cmp_eq_u32_e64 s[58:59], s51, v35\n"
    "s_and_b64 s[54:55], s[54:55], s[58:59]\n"
    "s_cmp_eq_u64 s[54:55], -1\n"
    "s_cbranch_scc1 .Lk_hdone_62\n"
    "s_sleep 4\n"
    "s_sub_u32 s52, s52, 1\n"
    "s_cmp_gt_i32 s52, 0\n"
    "s_cbranch_scc1 .Lk_hpoll_61\n"
    ".Lk_hdone_62:\n"
    "v_mfma_f32_16x16x32_bf16 v[36:39], v[24:27], v[32:35], 0\n"
    "s_nop 7\n"
    "v_add_f32_dpp v36, v36, v36 row_ror:4 row_mask:0xf bank_mask:0xf bound_ctrl:1\n"
    "v_add_f32_dpp v37, v37, v37 row_ror:4 row_mask:0xf bank_mask:0xf bound_ctrl:1\n"
    "v_add_f32_dpp v38, v38, v38 row_ror:4 row_mask:0xf bank_mask:0xf bound_ctrl:1\n"
    "v_add_f32_dpp v39, v39, v39 row_ror:4 row_mask:0xf bank_mask:0xf bound_ctrl:1\n"
    "v_add_f32_dpp v36, v36, v36 row_ror:8 row_mask:0xf bank_mask:0xf bound_ctrl:1\n"
    "v_add_f32_dpp v37, v37, v37 row_ror:8 row_mask:0xf bank_mask:0xf bound_ctrl:1\n"
    "v_add_f32_dpp v38, v38, v38 row_ror:8 row_mask:0xf bank_mask:0xf bound_ctrl:1\n"
    "v_add_f32_dpp v39, v39, v39 row_ror:8 row_mask:0xf bank_mask:0xf bound_ctrl:1\n"
    "v_mul_f32 v36, 0.5, v36\n"
    "v_mul_f32 v37, 0.5, v37\n"
    "v_mul_f32 v38, 0.5, v38\n"
    "v_mul_f32 v39, 0.5, v39\n"
    "v_cvt_pk_bf16_f32 v40, v36, v37\n"
    "v_cvt_pk_bf16_f32 v41, v38, v39\n"
    "v_lshlrev_b32 v36, 16, v40\n"
    "v_and_b32 v37, v9, v40\n"
    "v_lshlrev_b32 v38, 16, v41\n"
    "v_and_b32 v39, v9, v41\n"
    "v_lshlrev_b32 v40, 16, v20\n"
    "v_and_b32 v41, v9, v20\n"
    "v_lshlrev_b32 v42, 16, v21\n"
    "v_and_b32 v43, v9, v21\n"
    "v_pk_add_f32 v[36:37], v[36:37], v[40:41]\n"
    "v_pk_add_f32 v[38:39], v[38:39], v[42:43]\n"
    "v_cvt_pk_bf16_f32 v40, v36, v37\n"
    "v_cvt_pk_bf16_f32 v41, v38, v39\n"
    "v_lshlrev_b32 v16, 16, v40\n"
    "v_and_b32 v17, v9, v40\n"
    "v_lshlrev_b32 v18, 16, v41\n"
    "v_and_b32 v19, v9, v41\n"
    "s_add_u32 s50, s50, 1\n"
    "v_mov_b32 v23, s50\n"
    "buffer_load_dwordx4 v[64:67], v12, s[36:39], s10 offen sc1\n"
    "buffer_load_dwordx4 v[68:71], v12, s[36:39], s11 offen sc1\n"
    "buffer_load_dwordx4 v[72:75], v12, s[36:39], s12 offen sc1\n"
    "buffer_load_dwordx4 v[76:79], v12, s[36:39], s13 offen sc1\n"
    "s_nop 15\n"
    ".Lk_dpoll_63:\n"
    "buffer_load_dwordx4 v[80:83], v12, s[36:39], s10 offen sc1\n"
    "buffer_load_dwordx4 v[84:87], v12, s[36:39], s11 offen sc1\n"
    "buffer_load_dwordx4 v[88:91], v12, s[36:39], s12 offen sc1\n"
    "buffer_load_dwordx4 v[92:95], v12, s[36:39], s13 offen sc1\n"
    "s_waitcnt vmcnt(7)\n"
    "v_mfma_f32_16x16x32_bf16 v[48:51], v[24:27], v[64:67], 0\n"
    "v_cmp_eq_u32_e64 s[54:55], s51, v65\n"
    "v_cmp_eq_u32_e64 s[58:59], s51, v67\n"
    "s_and_b64 s[54:55], s[54:55], s[58:59]\n"
    "s_waitcnt vmcnt(6)\n"
    "v_mfma_f32_16x16x32_bf16 v[48:51], v[24:27], v[68:71], v[48:51]\n"
    "v_cmp_eq_u32_e64 s[56:57], s51, v69\n"
    "v_cmp_eq_u32_e64 s[58:59], s51, v71\n"
    "s_and_b64 s[56:57], s[56:57], s[58:59]\n"
    "s_and_b64 s[54:55], s[54:55], s[56:57]\n"
    "s_waitcnt vmcnt(5)\n"
    "v_mfma_f32_16x16x32_bf16 v[48:51], v[24:27], v[72:75], v[48:51]\n"
    "v_cmp_eq_u32_e64 s[56:57], s51, v73\n"
    "v_cmp_eq_u32_e64 s[58:59], s51, v75\n"
    "s_and_b64 s[56:57], s[56:57], s[58:59]\n"
    "s_and_b64 s[54:55], s[54:55], s[56:57]\n"
    "s_waitcnt vmcnt(4)\n"
    "v_mfma_f32_16x16x32_bf16 v[48:51], v[24:27], v[76:79], v[48:51]\n"
    "v_cmp_eq_u32_e64 s[56:57], s51, v77\n"
    "v_cmp_eq_u32_e64 s[58:59], s51, v79\n"
    "s_and_b64 s[56:57], s[56:57], s[58:59]\n"
    "s_mov_b32 s57, -1\n"
    "s_and_b64 s[54:55], s[54:55], s[56:57]\n"
    "s_cmp_eq_u64 s[54:55], -1\n"
    "s_cbranch_scc1 .Lk_finc_13\n"
    "buffer_load_dwordx4 v[64:67], v12, s[36:39], s10 offen sc1\n"
    "buffer_load_dwordx4 v[68:71], v12, s[36:39], s11 offen sc1\n"
    "buffer_load_dwordx4 v[72:75], v12, s[36:39], s12 offen sc1\n"
    "buffer_load_dwordx4 v[76:79], v12, s[36:39], s13 offen sc1\n"
    "s_waitcnt vmcnt(7)\n"
    "v_mfma_f32_16x16x32_bf16 v[48:51], v[24:27], v[80:83], 0\n"
    "v_cmp_eq_u32_e64 s[54:55], s51, v81\n"
    "v_cmp_eq_u32_e64 s[58:59], s51, v83\n"
    "s_and_b64 s[54:55], s[54:55], s[58:59]\n"
    "s_waitcnt vmcnt(6)\n"
    "v_mfma_f32_16x16x32_bf16 v[48:51], v[24:27], v[84:87], v[48:51]\n"
    "v_cmp_eq_u32_e64 s[56:57], s51, v85\n"
    "v_cmp_eq_u32_e64 s[58:59], s51, v87\n"
    "s_and_b64 s[56:57], s[56:57], s[58:59]\n"
    "s_and_b64 s[54:55], s[54:55], s[56:57]\n"
    "s_waitcnt vmcnt(5)\n"
    "v_mfma_f32_16x16x32_bf16 v[48:51], v[24:27], v[88:91], v[48:51]\n"
    "v_cmp_eq_u32_e64 s[56:57], s51, v89\n"
    "v_cmp_eq_u32_e64 s[58:59], s51, v91\n"
    "s_and_b64 s[56:57], s[56:57], s[58:59]\n"
    "s_and_b64 s[54:55], s[54:55], s[56:57]\n"
    "s_waitcnt vmcnt(4)\n"
    "v_mfma_f32_16x16x32_bf16 v[48:51], v[24:27], v[92:95], v[48:51]\n"
    "v_cmp_eq_u32_e64 s[56:57], s51, v93\n"
    "v_cmp_eq_u32_e64 s[58:59], s51, v95\n"
    "s_and_b64 s[56:57], s[56:57], s[58:59]\n"
    "s_mov_b32 s57, -1\n"
    "s_and_b64 s[54:55], s[54:55], s[56:57]\n"
    "s_cmp_eq_u64 s[54:55], -1\n"
    "s_cbranch_scc1 .Lk_finc_13\n"
    "s_sub_u32 s52, s52, 1\n"
    "s_cmp_gt_i32 s52, 0\n"
    "s_cbranch_scc1 .Lk_dpoll_63\n"
    ".Lk_finc_13:\n"
    "s_nop 1\n"
    "v_add_f32_dpp v48, v48, v48 row_ror:4 row_mask:0xf bank_mask:0xf bound_ctrl:1\n"
    "v_add_f32_dpp v49, v49, v49 row_ror:4 row_mask:0xf bank_mask:0xf bound_ctrl:1\n"
    "v_add_f32_dpp v50, v50, v50 row_ror:4 row_mask:0xf bank_mask:0xf bound_ctrl:1\n"
    "v_add_f32_dpp v51, v51, v51 row_ror:4 row_mask:0xf bank_mask:0xf bound_ctrl:1\n"
    "v_add_f32_dpp v48, v48, v48 row_ror:8 row_mask:0xf bank_mask:0xf bound_ctrl:1\n"
    "v_add_f32_dpp v49, v49, v49 row_ror:8 row_mask:0xf bank_mask:0xf bound_ctrl:1\n"
    "v_add_f32_dpp v50, v50, v50 row_ror:8 row_mask:0xf bank_mask:0xf bound_ctrl:1\n"
    "v_add_f32_dpp v51, v51, v51 row_ror:8 row_mask:0xf bank_mask:0xf bound_ctrl:1\n"
    "v_cvt_pk_bf16_f32 v40, v48, v49\n"
    "v_cvt_pk_bf16_f32 v41, v50, v51\n"
    "v_lshlrev_b32 v36, 16, v40\n"
    "v_and_b32 v37, v9, v40\n"
    "v_lshlrev_b32 v38, 16, v41\n"
    "v_and_b32 v39, v9, v41\n"
    "v_pk_add_f32 v[36:37], v[36:37], v[16:17]\n"
    "v_pk_add_f32 v[38:39], v[38:39], v[18:19]\n"
    "s_cbranch_execz .Lk_end_1\n"
    "s_mov_b64 exec, 15\n"
    "v_cvt_pk_bf16_f32 v40, v36, v37\n"
    "v_cvt_pk_bf16_f32 v41, v38, v39\n"
    "global_store_dwordx2 v11, v[40:41], s[34:35]\n"
    "s_mov_b64 exec, 1\n"
    "s_cmp_eq_u32 s8, 0\n"
    "s_cbranch_scc0 .Lk_end_1\n"
    "buffer_store_dword v23, off, s[36:39], 0 sc1\n"
    "s_branch .Lk_end_1\n"
    ".Lk_wo_8:\n"
    "s_lshl_b32 s6, s4, 19\n"
    "s_lshl_b32 s7, s3, 16\n"
    "s_add_u32 s6, s6, s7\n"
    "s_lshl_b32 s7, s8, 7\n"
    "s_add_u32 s6, s6, s7\n"
    "s_add_u32 s7, s6, 0x1000\n"
    "v_lshrrev_b32 v2, 3, v1\n"
    "v_lshlrev_b32 v2, 13, v2\n"
    "v_lshl_or_b32 v2, v3, 4, v2\n"
    "s_mov_b32 s40, s92\n"
    "s_and_b32 s41, s93, 0xffff\n"
    "s_barrier\n"
    "buffer_load_dwordx4 v[32:35], v2, s[40:43], s6 offen offset:0\n"
    "buffer_load_dwordx4 v[36:39], v2, s[40:43], s6 offen offset:1024\n"
    "buffer_load_dwordx4 v[40:43], v2, s[40:43], s6 offen offset:2048\n"
    "buffer_load_dwordx4 v[44:47], v2, s[40:43], s6 offen offset:3072\n"
    "buffer_load_dwordx4 v[48:51], v2, s[40:43], s7 offen offset:0\n"
    "buffer_load_dwordx4 v[52:55], v2, s[40:43], s7 offen offset:1024\n"
    "buffer_load_dwordx4 v[56:59], v2, s[40:43], s7 offen offset:2048\n"
    "buffer_load_dwordx4 v[60:63], v2, s[40:43], s7 offen offset:3072\n"
    "s_lshr_b32 s13, s4, 2\n"
    "s_lshl_b32 s13, s13, 14\n"
    "s_and_b32 s12, s4, 3\n"
    "s_lshl_b32 s12, s12, 11\n"
    "s_add_u32 s13, s13, s12\n"
    "s_add_u32 s13, s13, 638976\n"
    "s_lshl_b32 s12, s8, 8\n"
    "s_add_u32 s13, s13, s12\n"
    "s_lshl_b32 s12, s3, 6\n"
    "v_and_b32 v11, 3, v0\n"
    "v_lshlrev_b32 v11, 4, v11\n"
    "v_lshl_or_b32 v11, v5, 6, v11\n"
    "v_lshrrev_b32 v13, 2, v4\n"
    "v_lshl_or_b32 v11, v13, 11, v11\n"
    "s_lshr_b32 s9, s3, 1\n"
    "s_lshl_b32 s9, s9, 6\n"
    "v_lshlrev_b32 v10, 4, v13\n"
    "v_add_u32 v10, s9, v10\n"
    "s_lshl_b32 s9, s4, 16\n"
    "s_lshl_b32 s7, s3, 8\n"
    "s_add_u32 s9, s9, s7\n"
    "s_add_u32 s9, s9, 114688\n"
    "v_lshlrev_b32 v15, 2, v4\n"
    "v_lshl_or_b32 v14, v5, 10, v15\n"
    "v_lshl_or_b32 v15, v5, 13, v15\n"
    "v_add_u32 v14, s12, v14\n"
    "v_and_b32 v14, 31, v0\n"
    "v_lshlrev_b32 v14, 3, v14\n"
    "v_lshrrev_b32 v13, 5, v1\n"
    "v_lshl_or_b32 v15, v13, 13, v14\n"
    "v_lshl_or_b32 v14, v13, 2, v14\n"
    "s_lshl_b32 s7, s4, 3\n"
    "s_add_u32 s7, s7, s8\n"
    "s_lshl_b32 s7, s7, 4\n"
    "s_add_u32 s7, s7, 81920\n"
    "v_lshlrev_b32 v30, 13, v1\n"
    "v_add_u32 v30, s7, v30\n"
    "v_mov_b32 v29, s51\n"
    "v_mov_b32 v16, 0\n"
    "s_mov_b32 s53, 15\n"
    "s_waitcnt vmcnt(6)\n"
    "v_perm_b32 v64, v36, v32, s14\n"
    "v_perm_b32 v68, v36, v32, s15\n"
    "v_perm_b32 v72, v37, v33, s14\n"
    "v_perm_b32 v76, v37, v33, s15\n"
    "v_perm_b32 v80, v38, v34, s14\n"
    "v_perm_b32 v84, v38, v34, s15\n"
    "v_perm_b32 v88, v39, v35, s14\n"
    "v_perm_b32 v92, v39, v35, s15\n"
    "s_waitcnt vmcnt(4)\n"
    "v_perm_b32 v65, v44, v40, s14\n"
    "v_perm_b32 v69, v44, v40, s15\n"
    "v_perm_b32 v73, v45, v41, s14\n"
    "v_perm_b32 v77, v45, v41, s15\n"
    "v_perm_b32 v81, v46, v42, s14\n"
    "v_perm_b32 v85, v46, v42, s15\n"
    "v_perm_b32 v89, v47, v43, s14\n"
    "v_perm_b32 v93, v47, v43, s15\n"
    "s_waitcnt vmcnt(2)\n"
    "v_perm_b32 v66, v52, v48, s14\n"
    "v_perm_b32 v70, v52, v48, s15\n"
    "v_perm_b32 v74, v53, v49, s14\n"
    "v_perm_b32 v78, v53, v49, s15\n"
    "v_perm_b32 v82, v54, v50, s14\n"
    "v_perm_b32 v86, v54, v50, s15\n"
    "v_perm_b32 v90, v55, v51, s14\n"
    "v_perm_b32 v94, v55, v51, s15\n"
    "s_waitcnt vmcnt(0)\n"
    "v_perm_b32 v67, v60, v56, s14\n"
    "v_perm_b32 v71, v60, v56, s15\n"
    "v_perm_b32 v75, v61, v57, s14\n"
    "v_perm_b32 v79, v61, v57, s15\n"
    "v_perm_b32 v83, v62, v58, s14\n"
    "v_perm_b32 v87, v62, v58, s15\n"
    "v_perm_b32 v91, v63, v59, s14\n"
    "v_perm_b32 v95, v63, v59, s15\n"
    "s_mov_b32 s81, 15\n"
    "s_mov_b32 s12, 0\n"
    ".Lk_bpoll_64:\n"
    "ds_read_b32 v17, v16 offset:18944\n"
    "s_waitcnt lgkmcnt(0)\n"
    "v_readfirstlane_b32 s56, v17\n"
    "s_and_b32 s57, s56, s81\n"
    "s_cbranch_scc0 .Lk_idle_68\n"
    "s_and_b32 s6, s56, 0x2000\n"
    "s_add_u32 s6, s6, s9\n"
    "s_bitcmp1_b32 s57, 0\n"
    "s_cbranch_scc0 .Lk_sk_69\n"
    "s_add_u32 s7, s6, 0\n"
    "buffer_load_dwordx4 v[40:43], v11, s[36:39], s7 offen sc1\n"
    ".Lk_sk_69:\n"
    "s_bitcmp1_b32 s57, 1\n"
    "s_cbranch_scc0 .Lk_sk_70\n"
    "s_add_u32 s7, s6, 16384\n"
    "buffer_load_dwordx4 v[44:47], v11, s[36:39], s7 offen sc1\n"
    ".Lk_sk_70:\n"
    "s_bitcmp1_b32 s57, 2\n"
    "s_cbranch_scc0 .Lk_sk_71\n"
    "s_add_u32 s7, s6, 32768\n"
    "buffer_load_dwordx4 v[48:51], v11, s[36:39], s7 offen sc1\n"
    ".Lk_sk_71:\n"
    "s_bitcmp1_b32 s57, 3\n"
    "s_cbranch_scc0 .Lk_sk_72\n"
    "s_add_u32 s7, s6, 49152\n"
    "buffer_load_dwordx4 v[52:55], v11, s[36:39], s7 offen sc1\n"
    ".Lk_sk_72:\n"
    "s_or_b32 s12, s12, s57\n"
    "s_mov_b32 s11, 4\n"
    "s_andn2_b32 s81, s81, s57\n"
    "s_cbranch_scc1 .Lk_bpoll_64\n"
    ".Lk_woc_15:\n"
    "s_waitcnt vmcnt(0)\n"
    "ds_read_b32 v18, v16 offset:18944\n"
    "ds_read_b128 v[20:23], v10 offset:18432\n"
    "s_bitcmp1_b32 s12, 0\n"
    "s_cbranch_scc0 .Lk_sk_73\n"
    "v_cmp_eq_u32_e64 s[54:55], s51, v41\n"
    "v_cmp_eq_u32_e64 s[58:59], s51, v43\n"
    "s_and_b64 s[54:55], s[54:55], s[58:59]\n"
    "v_lshlrev_b32 v96, 16, v40\n"
    "v_and_b32 v97, v9, v40\n"
    "s_cmp_eq_u64 s[54:55], exec\n"
    "s_cselect_b32 s7, 1, 0\n"
    "s_andn2_b32 s53, s53, s7\n"
    "v_lshlrev_b32 v98, 16, v42\n"
    "v_and_b32 v99, v9, v42\n"
    ".Lk_sk_73:\n"
    "s_bitcmp1_b32 s12, 1\n"
    "s_cbranch_scc0 .Lk_sk_74\n"
    "v_cmp_eq_u32_e64 s[54:55], s51, v45\n"
    "v_cmp_eq_u32_e64 s[58:59], s51, v47\n"
    "s_and_b64 s[54:55], s[54:55], s[58:59]\n"
    "v_lshlrev_b32 v100, 16, v44\n"
    "v_and_b32 v101, v9, v44\n"
    "s_cmp_eq_u64 s[54:55], exec\n"
    "s_cselect_b32 s7, 2, 0\n"
    "s_andn2_b32 s53, s53, s7\n"
    "v_lshlrev_b32 v102, 16, v46\n"
    "v_and_b32 v103, v9, v46\n"
    ".Lk_sk_74:\n"
    "s_bitcmp1_b32 s12, 2\n"
    "s_cbranch_scc0 .Lk_sk_75\n"
    "v_cmp_eq_u32_e64 s[54:55], s51, v49\n"
    "v_cmp_eq_u32_e64 s[58:59], s51, v51\n"
    "s_and_b64 s[54:55], s[54:55], s[58:59]\n"
    "v_lshlrev_b32 v104, 16, v48\n"
    "v_and_b32 v105, v9, v48\n"
    "s_cmp_eq_u64 s[54:55], exec\n"
    "s_cselect_b32 s7, 4, 0\n"
    "s_andn2_b32 s53, s53, s7\n"
    "v_lshlrev_b32 v106, 16, v50\n"
    "v_and_b32 v107, v9, v50\n"
    ".Lk_sk_75:\n"
    "s_bitcmp1_b32 s12, 3\n"
    "s_cbranch_scc0 .Lk_sk_76\n"
    "v_cmp_eq_u32_e64 s[54:55], s51, v53\n"
    "v_cmp_eq_u32_e64 s[58:59], s51, v55\n"
    "s_and_b64 s[54:55], s[54:55], s[58:59]\n"
    "v_lshlrev_b32 v108, 16, v52\n"
    "v_and_b32 v109, v9, v52\n"
    "s_cmp_eq_u64 s[54:55], exec\n"
    "s_cselect_b32 s7, 8, 0\n"
    "s_andn2_b32 s53, s53, s7\n"
    "v_lshlrev_b32 v110, 16, v54\n"
    "v_and_b32 v111, v9, v54\n"
    ".Lk_sk_76:\n"
    "s_cmp_eq_u32 s53, 0\n"
    "s_cbranch_scc1 .Lk_bdone_65\n"
    "s_mov_b32 s81, s53\n"
    "s_mov_b32 s12, 0\n"
    "s_branch .Lk_nb_66\n"
    ".Lk_idle_68:\n"
    "s_cmp_eq_u32 s12, 0\n"
    "s_cbranch_scc1 .Lk_nb_66\n"
    "s_sub_u32 s11, s11, 1\n"
    "s_cbranch_scc1 .Lk_woc_15\n"
    ".Lk_nb_66:\n"
    "s_sub_u32 s52, s52, 1\n"
    "s_cmp_gt_i32 s52, 0\n"
    "s_cbranch_scc1 .Lk_bpoll_64\n"
    ".Lk_wpoll_67:\n"
    "ds_read_b32 v18, v16 offset:18944\n"
    "ds_read_b128 v[20:23], v10 offset:18432\n"
    "s_sub_u32 s52, s52, 1\n"
    "s_cmp_gt_i32 s52, 0\n"
    "s_cselect_b64 exec, exec, 0\n"
    ".Lk_bdone_65:\n"
    "s_waitcnt lgkmcnt(0)\n"
    "v_and_b32 v18, 16, v18\n"
    "v_cmp_eq_u32 vcc, 0, v18\n"
    "s_cbranch_vccnz .Lk_wpoll_67\n"
    "s_mov_b64 exec, s[88:89]\n"
    "v_pk_mul_f32 v[32:33], v[96:97], v[20:21] op_sel_hi:[1,0]\n"
    "v_pk_mul_f32 v[34:35], v[98:99], v[20:21] op_sel_hi:[1,0]\n"
    "v_pk_fma_f32 v[32:33], v[100:101], v[20:21], v[32:33] op_sel:[0,1,0] op_sel_hi:[1,1,1]\n"
    "v_pk_fma_f32 v[34:35], v[102:103], v[20:21], v[34:35] op_sel:[0,1,0] op_sel_hi:[1,1,1]\n"
    "v_pk_fma_f32 v[32:33], v[104:105], v[22:23], v[32:33] op_sel_hi:[1,0,1]\n"
    "v_pk_fma_f32 v[34:35], v[106:107], v[22:23], v[34:35] op_sel_hi:[1,0,1]\n"
    "v_pk_fma_f32 v[32:33], v[108:109], v[22:23], v[32:33] op_sel:[0,1,0] op_sel_hi:[1,1,1]\n"
    "v_pk_fma_f32 v[34:35], v[110:111], v[22:23], v[34:35] op_sel:[0,1,0] op_sel_hi:[1,1,1]\n"
    "s_nop 0\n"
    "v_add_f32_dpp v32, v32, v32 row_ror:4 row_mask:0xf bank_mask:0xf bound_ctrl:1\n"
    "v_add_f32_dpp v33, v33, v33 row_ror:4 row_mask:0xf bank_mask:0xf bound_ctrl:1\n"
    "v_add_f32_dpp v34, v34, v34 row_ror:4 row_mask:0xf bank_mask:0xf bound_ctrl:1\n"
    "v_add_f32_dpp v35, v35, v35 row_ror:4 row_mask:0xf bank_mask:0xf bound_ctrl:1\n"
    "v_add_f32_dpp v32, v32, v32 row_ror:8 row_mask:0xf bank_mask:0xf bound_ctrl:1\n"
    "v_add_f32_dpp v33, v33, v33 row_ror:8 row_mask:0xf bank_mask:0xf bound_ctrl:1\n"
    "v_add_f32_dpp v34, v34, v34 row_ror:8 row_mask:0xf bank_mask:0xf bound_ctrl:1\n"
    "v_add_f32_dpp v35, v35, v35 row_ror:8 row_mask:0xf bank_mask:0xf bound_ctrl:1\n"
    "v_cvt_pk_bf16_f32 v36, v32, v33\n"
    "v_cvt_pk_bf16_f32 v37, v34, v35\n"
    "s_cmp_lt_u32 s3, 4\n"
    "v_mov_b32_dpp v38, v36 quad_perm:[1,0,3,2] row_mask:0xf bank_mask:0xf bound_ctrl:1\n"
    "v_mov_b32_dpp v39, v37 quad_perm:[1,0,3,2] row_mask:0xf bank_mask:0xf bound_ctrl:1\n"
    "s_nop 1\n"
    "v_mfma_f32_16x16x32_bf16 v[96:99], v[36:39], v[64:67], 0\n"
    "v_mfma_f32_16x16x32_bf16 v[100:103], v[36:39], v[68:71], 0\n"
    "v_mfma_f32_16x16x32_bf16 v[104:107], v[36:39], v[72:75], 0\n"
    "v_mfma_f32_16x16x32_bf16 v[108:111], v[36:39], v[76:79], 0\n"
    "v_mfma_f32_16x16x32_bf16 v[112:115], v[36:39], v[80:83], 0\n"
    "v_mfma_f32_16x16x32_bf16 v[116:119], v[36:39], v[84:87], 0\n"
    "v_mfma_f32_16x16x32_bf16 v[120:123], v[36:39], v[88:91], 0\n"
    "v_mfma_f32_16x16x32_bf16 v[124:127], v[36:39], v[92:95], 0\n"
    "s_mov_b32 exec_hi, 0\n"
    "s_min_u32 exec_lo, s88, 0xffff\n"
    "s_cmp_lt_u32 s3, 1\n"
    "v_cndmask_b32_e64 v40, v96, v98, s[48:49]\n"
    "v_cndmask_b32_e64 v41, v100, v102, s[48:49]\n"
    "v_cndmask_b32_e64 v42, v104, v106, s[48:49]\n"
    "v_cndmask_b32_e64 v43, v108, v110, s[48:49]\n"
    "ds_write_b128 v12, v[40:43]\n"
    "v_cndmask_b32_e64 v44, v112, v114, s[48:49]\n"
    "v_cndmask_b32_e64 v45, v116, v118, s[48:49]\n"
    "v_cndmask_b32_e64 v46, v120, v122, s[48:49]\n"
    "v_cndmask_b32_e64 v47, v124, v126, s[48:49]\n"
    "ds_write_b128 v12, v[44:47] offset:16\n"
    "s_mov_b64 exec, s[88:89]\n"
    "s_waitcnt lgkmcnt(0)\n"
    "s_cbranch_execz .Lk_nb_77\n"
    "s_barrier\n"
    ".Lk_nb_77:\n"
    "s_cbranch_scc0 .Lk_end_1\n"
    "ds_read2st64_b32 v[96:97], v14 offset0:0 offset1:1\n"
    "ds_read2st64_b32 v[98:99], v14 offset0:2 offset1:3\n"
    "ds_read2st64_b32 v[100:101], v14 offset0:4 offset1:5\n"
    "ds_read2st64_b32 v[102:103], v14 offset0:6 offset1:7\n"
    "ds_read2st64_b32 v[104:105], v14 offset0:8 offset1:9\n"
    "ds_read2st64_b32 v[106:107], v14 offset0:10 offset1:11\n"
    "ds_read2st64_b32 v[108:109], v14 offset0:12 offset1:13\n"
    "ds_read2st64_b32 v[110:111], v14 offset0:14 offset1:15\n"
    "s_waitcnt lgkmcnt(6)\n"
    "v_pk_add_f32 v[96:97], v[96:97], v[98:99]\n"
    "s_waitcnt lgkmcnt(4)\n"
    "v_pk_add_f32 v[100:101], v[100:101], v[102:103]\n"
    "s_waitcnt lgkmcnt(2)\n"
    "v_pk_add_f32 v[104:105], v[104:105], v[106:107]\n"
    "v_pk_add_f32 v[96:97], v[96:97], v[100:101]\n"
    "s_waitcnt lgkmcnt(0)\n"
    "v_pk_add_f32 v[108:109], v[108:109], v[110:111]\n"
    "v_pk_add_f32 v[104:105], v[104:105], v[108:109]\n"
    "v_pk_add_f32 v[96:97], v[96:97], v[104:105]\n"
    "v_add_f32 v32, v96, v97\n"
    "v_mov_b32 v33, v32\n"
    "s_cbranch_execz .Lk_end_1\n"
    "s_nop 0\n"
    "v_permlane32_swap_b32 v32, v33\n"
    "v_cvt_pk_bf16_f32 v28, v32, v33\n"
    "buffer_store_dwordx2 v[28:29], v15, s[36:39], s13 offen sc1\n"
    "s_branch .Lk_end_1\n"
    ".Lk_spare_11:\n"
    "s_cmp_eq_u32 s2, 249\n"
    "s_cbranch_scc0 .Lk_end_1\n"
    "s_lshl_b32 s6, s3, 9\n"
    "s_add_u32 s6, s6, 2048\n"
    "s_mov_b32 s10, -1\n"
    "s_mov_b32 s11, -1\n"
    "s_lshl_b32 s7, s3, 3\n"
    "v_mov_b32 v18, s7\n"
    "v_lshlrev_b32 v19, 3, v3\n"
    "s_getreg_b32 s12, hwreg(HW_REG_XCC_ID, 2, 1)\n"
    "s_movk_i32 s13, 1024\n"
    "s_waitcnt lgkmcnt(0)\n"
    "s_barrier\n"
    "s_mul_i32 s51, s50, 0x9e3779b1\n"
    "s_add_u32 s51, s51, 0x7f4a7c15\n"
    "s_and_b32 s51, s51, 0xbfffbfff\n"
    "s_mov_b32 s9, 2\n"
    ".Lk_ora_78:\n"
    "s_add_u32 s7, s6, 0\n"
    "s_memtime s[82:83]\n"
    "s_waitcnt lgkmcnt(0)\n"
    "s_load_dword s84, s[30:31], s7\n"
    "s_waitcnt lgkmcnt(0)\n"
    "s_memtime s[86:87]\n"
    "s_waitcnt lgkmcnt(0)\n"
    "s_sub_u32 s86, s86, s82\n"
    "s_min_u32 s10, s10, s86\n"
    "s_add_u32 s7, s6, 8192\n"
    "s_memtime s[82:83]\n"
    "s_waitcnt lgkmcnt(0)\n"
    "s_load_dword s84, s[30:31], s7\n"
    "s_waitcnt lgkmcnt(0)\n"
    "s_memtime s[86:87]\n"
    "s_waitcnt lgkmcnt(0)\n"
    "s_sub_u32 s86, s86, s82\n"
    "s_min_u32 s11, s11, s86\n"
    "s_add_u32 s6, s6, 128\n"
    "s_sub_u32 s9, s9, 1\n"
    "s_cmp_gt_i32 s9, 0\n"
    "s_cbranch_scc1 .Lk_ora_78\n"
    "v_mov_b32 v16, s10\n"
    "v_mov_b32 v17, s11\n"
    "ds_write_b64 v18, v[16:17]\n"
    "s_waitcnt lgkmcnt(0)\n"
    "s_barrier\n"
    "s_cmp_eq_u32 s3, 0\n"
    "s_cbranch_scc0 .Lk_ora2_79\n"
    "ds_read_b64 v[16:17], v19\n"
    "s_waitcnt lgkmcnt(0)\n"
    "v_min_u32_dpp v16, v16, v16 quad_perm:[1,0,3,2] row_mask:0xf bank_mask:0xf bound_ctrl:1\n"
    "v_min_u32_dpp v17, v17, v17 quad_perm:[1,0,3,2] row_mask:0xf bank_mask:0xf bound_ctrl:1\n"
    "s_nop 0\n"
    "v_min_u32_dpp v16, v16, v16 quad_perm:[2,3,0,1] row_mask:0xf bank_mask:0xf bound_ctrl:1\n"
    "v_min_u32_dpp v17, v17, v17 quad_perm:[2,3,0,1] row_mask:0xf bank_mask:0xf bound_ctrl:1\n"
    "s_nop 0\n"
    "v_min_u32_dpp v16, v16, v16 row_half_mirror row_mask:0xf bank_mask:0xf bound_ctrl:1\n"
    "v_min_u32_dpp v17, v17, v17 row_half_mirror row_mask:0xf bank_mask:0xf bound_ctrl:1\n"
    "v_readfirstlane_b32 s10, v16\n"
    "v_readfirstlane_b32 s11, v17\n"
    "s_cmp_lt_u32 s11, s10\n"
    "s_cselect_b32 s7, 1, 0\n"
    "s_xor_b32 s7, s7, s12\n"
    "v_mov_b32 v20, s7\n"
    "v_mov_b32 v21, s51\n"
    "s_mov_b64 exec, 1\n"
    "buffer_store_dwordx2 v[20:21], off, s[36:39], s13 sc1\n"
    "s_mov_b64 exec, -1\n"
    ".Lk_ora2_79:\n"
    "s_cmp_eq_u32 s13, 1024\n"
    "s_cbranch_scc0 .Lk_end_1\n"
    "s_movk_i32 s13, 1152\n"
    "s_mov_b32 s9, 2\n"
    ".Lk_dl_80:\n"
    "s_memrealtime s[62:63]\n"
    "s_waitcnt lgkmcnt(0)\n"
    "s_sub_u32 s62, s62, s32\n"
    "s_cmp_lt_u32 s62, 390\n"
    "s_cbranch_scc0 .Lk_dd_81\n"
    "s_sleep 1\n"
    "s_branch .Lk_dl_80\n"
    ".Lk_dd_81:\n"
    "s_branch .Lk_ora_78\n"
    ".Lk_upaux_7:\n"
    "s_cmp_eq_u32 s2, 249\n"
    "s_cbranch_scc0 .Lk_no_82\n"
    "s_waitcnt vmcnt(1)\n"
    "s_barrier\n"
    "s_branch .Lk_end_1\n"
    ".Lk_no_82:\n"
    "s_waitcnt lgkmcnt(0)\n"
    "s_load_dwordx8 s[92:99], s[0:1], 0x40\n"
    "s_load_dwordx2 s[34:35], s[0:1], 0x60\n"
    "s_mov_b32 s42, 0x7fffffff\n"
    "s_mov_b32 s43, 0x20000\n"
    "s_mov_b32 s36, s30\n"
    "s_and_b32 s37, s31, 0xffff\n"
    "s_mov_b32 s38, 901120\n"
    "s_mov_b32 s39, 0x20000\n"
    "s_load_dword s50, s[30:31], 0x0\n"
    "s_mov_b64 s[88:89], -1\n"
    "s_mov_b32 s52, 0x20000\n"
    "v_and_b32 v4, 15, v1\n"
    "v_lshrrev_b32 v5, 4, v1\n"
    "v_mov_b32 v9, 0xffff0000\n"
    "s_mov_b32 s44, 0xaaaaaaaa\n"
    "s_mov_b32 s45, 0xaaaaaaaa\n"
    "s_mov_b32 s46, 0xcccccccc\n"
    "s_mov_b32 s47, 0xcccccccc\n"
    "s_mov_b32 s48, 0xff00ff00\n"
    "s_mov_b32 s49, 0xff00ff00\n"
    "s_mov_b32 s14, 0x05040100\n"
    "s_mov_b32 s15, 0x07060302\n"
    "s_mov_b64 exec, 0\n"
    "s_mov_b64 s[88:89], 0\n"
    "s_mov_b32 s3, 0\n"
    "s_getpc_b64 s[90:91]\n"
    ".Lk_pc_84:\n"
    "s_add_u32 s90, s90, (.Lk_ret_83-.Lk_pc_84)\n"
    "s_addc_u32 s91, s91, 0\n"
    "s_branch .Lk_upc_16\n"
    ".Lk_ret_83:\n"
    "s_mov_b64 s[88:89], -1\n"
    "s_mov_b64 exec, -1\n"
    "s_mov_b32 s3, 8\n"
    "s_nop 7\n"
    "s_waitcnt lgkmcnt(0)\n"
    "s_and_b32 s8, s5, 7\n"
    "s_cmp_lt_u32 s8, 4\n"
    "s_cselect_b64 s[40:41], s[94:95], s[96:97]\n"
    "s_mul_i32 s82, s4, 7\n"
    "s_add_u32 s82, s82, 1\n"
    "s_lshr_b32 s82, s82, 1\n"
    "s_lshr_b32 s83, s40, 7\n"
    "s_add_u32 s83, s83, s82\n"
    "s_sub_u32 s83, 3, s83\n"
    "s_and_b32 s84, s83, 3\n"
    "s_add_u32 s82, s82, s84\n"
    "s_lshl_b32 s82, s82, 7\n"
    "s_bfe_u32 s83, s83, 0x10002\n"
    "s_and_b32 s84, s8, 3\n"
    "s_lshl_b32 s84, s84, 7\n"
    "s_add_u32 s83, s83, s84\n"
    "s_mul_i32 s83, s83, 3584\n"
    "s_add_u32 s82, s82, s83\n"
    "s_mov_b32 s42, 1835008\n"
    "v_mul_u32_u24 v13, 7168, v1\n"
    "s_and_b32 s41, s41, 0xffff\n"
    ".Lk_dl_85:\n"
    "s_memrealtime s[62:63]\n"
    "s_waitcnt lgkmcnt(0)\n"
    "s_sub_u32 s62, s62, s32\n"
    "s_cmp_lt_u32 s62, 190\n"
    "s_cbranch_scc0 .Lk_dd_86\n"
    "s_sleep 1\n"
    "s_branch .Lk_dl_85\n"
    ".Lk_dd_86:\n"
    "buffer_load_dword v48, v13, s[40:43], s82 offen\n"
    "s_and_b32 s6, s4, 1\n"
    "s_lshl_b32 s6, s6, 1\n"
    "s_add_u32 s6, s6, s5\n"
    "s_cmp_lt_u32 s6, 32\n"
    "s_cbranch_scc0 .Lk_end_1\n"
    "s_mul_i32 s51, s50, 0x9e3779b1\n"
    "s_add_u32 s51, s51, 0x7f4a7c15\n"
    "s_and_b32 s51, s51, 0xbfffbfff\n"
    ".Lk_dl_87:\n"
    "s_memrealtime s[62:63]\n"
    "s_waitcnt lgkmcnt(0)\n"
    "s_sub_u32 s62, s62, s32\n"
    "s_cmp_lt_u32 s62, 480\n"
    "s_cbranch_scc0 .Lk_dd_88\n"
    "s_sleep 1\n"
    "s_branch .Lk_dl_87\n"
    ".Lk_dd_88:\n"
    ".Lk_ppoll_89:\n"
    "buffer_load_dwordx2 v[160:161], off, s[36:39], 0 offset:1152 sc1\n"
    ".Lk_pchk_91:\n"
    "s_waitcnt vmcnt(0)\n"
    "v_readfirstlane_b32 s82, v161\n"
    "v_readfirstlane_b32 s80, v160\n"
    "s_cmp_eq_u32 s82, s51\n"
    "s_cbranch_scc1 .Lk_pdone_90\n"
    "s_sleep 2\n"
    "s_sub_u32 s52, s52, 1\n"
    "s_cmp_gt_i32 s52, 0\n"
    "s_cbranch_scc1 .Lk_ppoll_89\n"
    ".Lk_pdone_90:\n"
    "s_getreg_b32 s82, hwreg(HW_REG_XCC_ID, 2, 1)\n"
    "s_xor_b32 s80, s80, s82\n"
    "s_and_b32 s80, s80, 1\n"
    "s_lshl_b32 s80, s80, 13\n"
    "v_mov_b32 v20, 0\n"
    "v_mov_b32 v21, s80\n"
    "ds_write_b32 v20, v21 offset:18944\n"
    "s_branch .Lk_end_1\n"
    ".Lk_up_9:\n"
    "s_mul_i32 s9, s4, 7\n"
    "s_and_b32 s10, s4, 1\n"
    "s_add_u32 s9, s9, s10\n"
    "s_add_u32 s9, s9, s8\n"
    "s_sub_u32 s9, s9, 8\n"
    "s_and_b32 s10, s9, 1\n"
    "s_lshl_b32 s10, s10, 2\n"
    "s_add_u32 s10, s10, s3\n"
    "s_and_b32 s10, s10, 7\n"
    "s_mul_i32 s6, s10, 229376\n"
    "s_lshl_b32 s7, s9, 6\n"
    "s_add_u32 s6, s6, s7\n"
    "v_lshrrev_b32 v2, 3, v1\n"
    "v_mul_u32_u24 v2, 28672, v2\n"
    "v_and_b32 v10, 3, v0\n"
    "v_lshl_add_u32 v2, v10, 4, v2\n"
    "v_add_u32 v2, s6, v2\n"
    "s_mov_b32 s56, 0xf0f0f0f0\n"
    "s_mov_b32 s57, 0xf0f0f0f0\n"
    "s_waitcnt lgkmcnt(0)\n"
    "v_mov_b32 v16, s94\n"
    "v_mov_b32 v17, s95\n"
    "v_mov_b32 v18, s96\n"
    "v_mov_b32 v19, s97\n"
    "v_cndmask_b32_e64 v16, v16, v18, s[56:57]\n"
    "v_cndmask_b32_e64 v17, v17, v19, s[56:57]\n"
    "v_add_co_u32 v16, vcc, v16, v2\n"
    "s_nop 1\n"
    "v_addc_co_u32 v17, vcc, 0, v17, vcc\n"
    "s_mov_b64 s[6:7], 7168\n"
    "v_lshl_add_u64 v[18:19], v[16:17], 0, s[6:7]\n"
    "v_lshl_add_u64 v[20:21], v[18:19], 0, s[6:7]\n"
    "v_lshl_add_u64 v[22:23], v[20:21], 0, s[6:7]\n"
    "s_lshl_b32 s6, s10, 7\n"
    "v_lshlrev_b32 v11, 3, v10\n"
    "v_lshl_or_b32 v11, v5, 5, v11\n"
    "v_add_u32 v13, s6, v11\n"
    ".Lk_dl_92:\n"
    "s_memrealtime s[62:63]\n"
    "s_waitcnt lgkmcnt(0)\n"
    "s_sub_u32 s62, s62, s32\n"
    "s_cmp_lt_u32 s62, 250\n"
    "s_cbranch_scc0 .Lk_dd_93\n"
    "s_sleep 1\n"
    "s_branch .Lk_dl_92\n"
    ".Lk_dd_93:\n"
    "global_load_dwordx2 v[24:25], v13, s[28:29]\n"
    "s_cmp_lt_u32 s3, 4\n"
    "s_cbranch_scc0 .Lk_now2_94\n"
    "s_lshl_b32 s6, s9, 15\n"
    "s_lshl_b32 s7, s3, 8\n"
    "s_add_u32 s6, s6, s7\n"
    "s_add_u32 s7, s6, 0x1000\n"
    "v_lshlrev_b32 v2, 4, v4\n"
    "v_lshl_or_b32 v2, v5, 13, v2\n"
    "s_mov_b32 s40, s98\n"
    "s_and_b32 s41, s99, 0xffff\n"
    "buffer_load_dwordx4 v[96:99], v2, s[40:43], s6 offen offset:0\n"
    "buffer_load_dwordx4 v[100:103], v2, s[40:43], s6 offen offset:1024\n"
    "buffer_load_dwordx4 v[104:107], v2, s[40:43], s6 offen offset:2048\n"
    "buffer_load_dwordx4 v[108:111], v2, s[40:43], s6 offen offset:3072\n"
    "buffer_load_dwordx4 v[112:115], v2, s[40:43], s7 offen offset:0\n"
    "buffer_load_dwordx4 v[116:119], v2, s[40:43], s7 offen offset:1024\n"
    "buffer_load_dwordx4 v[120:123], v2, s[40:43], s7 offen offset:2048\n"
    "buffer_load_dwordx4 v[124:127], v2, s[40:43], s7 offen offset:3072\n"
    ".Lk_now2_94:\n"
    "global_load_dwordx4 v[32:35], v[16:17], off offset:0\n"
    "global_load_dwordx4 v[36:39], v[16:17], off offset:3584\n"
    "global_load_dwordx4 v[40:43], v[18:19], off offset:0\n"
    "global_load_dwordx4 v[44:47], v[18:19], off offset:3584\n"
    "global_load_dwordx4 v[48:51], v[20:21], off offset:0\n"
    "global_load_dwordx4 v[52:55], v[20:21], off offset:3584\n"
    "global_load_dwordx4 v[56:59], v[22:23], off offset:0\n"
    "global_load_dwordx4 v[60:63], v[22:23], off offset:3584\n"
    "s_waitcnt lgkmcnt(0)\n"
    "s_mul_i32 s51, s50, 0x9e3779b1\n"
    "s_add_u32 s51, s51, 0x7f4a7c15\n"
    "s_and_b32 s51, s51, 0xbfffbfff\n"
    "v_lshrrev_b32 v12, 3, v4\n"
    "v_lshlrev_b32 v12, 8, v12\n"
    "v_lshl_or_b32 v12, v10, 6, v12\n"
    "v_and_b32 v13, 4, v0\n"
    "v_lshl_or_b32 v12, v13, 3, v12\n"
    "s_lshl_b32 s6, s3, 9\n"
    "v_add_u32 v12, s6, v12\n"
    "s_lshl_b32 s6, s3, 7\n"
    "v_lshlrev_b32 v14, 3, v4\n"
    "v_add_u32 v14, s6, v14\n"
    "v_lshlrev_b32 v11, 1, v11\n"
    "v_lshrrev_b32 v13, 2, v4\n"
    "v_lshl_or_b32 v11, v13, 11, v11\n"
    "s_lshl_b32 s11, s10, 8\n"
    "s_add_u32 s11, s11, 638976\n"
    "v_mov_b32 v16, 0x3f803f80\n"
    "v_mov_b32 v17, 0x3f803f80\n"
    "v_mov_b32 v18, 0x3f803f80\n"
    "v_mov_b32 v19, 0x3f803f80\n"
    "v_mov_b32 v20, 0x3f80\n"
    "v_mov_b32 v21, 0x3f800000\n"
    "v_lshlrev_b32 v22, 2, v1\n"
    "v_lshrrev_b32 v23, 1, v4\n"
    "v_lshlrev_b32 v23, 7, v23\n"
    "v_and_b32 v13, 1, v0\n"
    "v_mul_u32_u24 v13, 80, v13\n"
    "v_add_u32 v23, v13, v23\n"
    "v_mov_b32 v29, s51\n"
    "v_mov_b32 v31, s51\n"
    "v_mov_b32 v27, 3216550459\n"
    "v_lshrrev_b32 v13, 5, v1\n"
    "v_lshlrev_b32 v13, 4, v13\n"
    "v_lshl_or_b32 v13, v4, 5, v13\n"
    "v_and_b32 v15, 1, v5\n"
    "v_lshl_or_b32 v15, v15, 13, v13\n"
    "s_lshr_b32 s13, s9, 2\n"
    "s_lshl_b32 s13, s13, 14\n"
    "s_and_b32 s6, s9, 3\n"
    "s_lshl_b32 s6, s6, 11\n"
    "s_add_u32 s13, s13, s6\n"
    "s_lshl_b32 s6, s3, 9\n"
    "s_add_u32 s13, s13, s6\n"
    "s_add_u32 s13, s13, 671744\n"
    "s_waitcnt vmcnt(8)\n"
    "s_cmp_lt_u32 s3, 4\n"
    "s_cbranch_scc0 .Lk_now3_95\n"
    "v_perm_b32 v128, v100, v96, s14\n"
    "v_perm_b32 v132, v100, v96, s15\n"
    "v_perm_b32 v136, v101, v97, s14\n"
    "v_perm_b32 v140, v101, v97, s15\n"
    "v_perm_b32 v144, v102, v98, s14\n"
    "v_perm_b32 v148, v102, v98, s15\n"
    "v_perm_b32 v152, v103, v99, s14\n"
    "v_perm_b32 v156, v103, v99, s15\n"
    "v_perm_b32 v129, v108, v104, s14\n"
    "v_perm_b32 v133, v108, v104, s15\n"
    "v_perm_b32 v137, v109, v105, s14\n"
    "v_perm_b32 v141, v109, v105, s15\n"
    "v_perm_b32 v145, v110, v106, s14\n"
    "v_perm_b32 v149, v110, v106, s15\n"
    "v_perm_b32 v153, v111, v107, s14\n"
    "v_perm_b32 v157, v111, v107, s15\n"
    "v_perm_b32 v130, v116, v112, s14\n"
    "v_perm_b32 v134, v116, v112, s15\n"
    "v_perm_b32 v138, v117, v113, s14\n"
    "v_perm_b32 v142, v117, v113, s15\n"
    "v_perm_b32 v146, v118, v114, s14\n"
    "v_perm_b32 v150, v118, v114, s15\n"
    "v_perm_b32 v154, v119, v115, s14\n"
    "v_perm_b32 v158, v119, v115, s15\n"
    "v_perm_b32 v131, v124, v120, s14\n"
    "v_perm_b32 v135, v124, v120, s15\n"
    "v_perm_b32 v139, v125, v121, s14\n"
    "v_perm_b32 v143, v125, v121, s15\n"
    "v_perm_b32 v147, v126, v122, s14\n"
    "v_perm_b32 v151, v126, v122, s15\n"
    "v_perm_b32 v155, v127, v123, s14\n"
    "v_perm_b32 v159, v127, v123, s15\n"
    ".Lk_now3_95:\n"
    "s_mov_b32 s56, 0x000f000f\n"
    "s_mov_b32 s57, 0x000f000f\n"
    "v_cndmask_b32_e64 v24, 0, v24, s[56:57]\n"
    "v_cndmask_b32_e64 v25, 0, v25, s[56:57]\n"
    "s_waitcnt vmcnt(6)\n"
    "v_perm_b32 v64, v36, v32, s14\n"
    "v_perm_b32 v68, v36, v32, s15\n"
    "v_perm_b32 v72, v37, v33, s14\n"
    "v_perm_b32 v76, v37, v33, s15\n"
    "v_perm_b32 v80, v38, v34, s14\n"
    "v_perm_b32 v84, v38, v34, s15\n"
    "v_perm_b32 v88, v39, v35, s14\n"
    "v_perm_b32 v92, v39, v35, s15\n"
    "s_waitcnt vmcnt(4)\n"
    "v_perm_b32 v65, v44, v40, s14\n"
    "v_perm_b32 v69, v44, v40, s15\n"
    "v_perm_b32 v73, v45, v41, s14\n"
    "v_perm_b32 v77, v45, v41, s15\n"
    "v_perm_b32 v81, v46, v42, s14\n"
    "v_perm_b32 v85, v46, v42, s15\n"
    "v_perm_b32 v89, v47, v43, s14\n"
    "v_perm_b32 v93, v47, v43, s15\n"
    "s_waitcnt vmcnt(2)\n"
    "v_perm_b32 v66, v52, v48, s14\n"
    "v_perm_b32 v70, v52, v48, s15\n"
    "v_perm_b32 v74, v53, v49, s14\n"
    "v_perm_b32 v78, v53, v49, s15\n"
    "v_perm_b32 v82, v54, v50, s14\n"
    "v_perm_b32 v86, v54, v50, s15\n"
    "v_perm_b32 v90, v55, v51, s14\n"
    "v_perm_b32 v94, v55, v51, s15\n"
    "s_waitcnt vmcnt(0)\n"
    "v_perm_b32 v67, v60, v56, s14\n"
    "v_perm_b32 v71, v60, v56, s15\n"
    "v_perm_b32 v75, v61, v57, s14\n"
    "v_perm_b32 v79, v61, v57, s15\n"
    "v_perm_b32 v83, v62, v58, s14\n"
    "v_perm_b32 v87, v62, v58, s15\n"
    "v_perm_b32 v91, v63, v59, s14\n"
    "v_perm_b32 v95, v63, v59, s15\n"
    "v_mfma_f32_16x16x32_bf16 v[96:99], v[16:19], v[64:67], 0\n"
    "v_mfma_f32_16x16x32_bf16 v[100:103], v[16:19], v[68:71], 0\n"
    "v_mfma_f32_16x16x32_bf16 v[104:107], v[16:19], v[72:75], 0\n"
    "v_mfma_f32_16x16x32_bf16 v[108:111], v[16:19], v[76:79], 0\n"
    "v_mfma_f32_16x16x32_bf16 v[112:115], v[16:19], v[80:83], 0\n"
    "v_mfma_f32_16x16x32_bf16 v[116:119], v[16:19], v[84:87], 0\n"
    "v_mfma_f32_16x16x32_bf16 v[120:123], v[16:19], v[88:91], 0\n"
    "v_mfma_f32_16x16x32_bf16 v[124:127], v[16:19], v[92:95], 0\n"
    "v_lshlrev_b32 v48, 16, v24\n"
    "v_and_b32 v49, v9, v24\n"
    "v_lshlrev_b32 v50, 16, v25\n"
    "v_and_b32 v51, v9, v25\n"
    "s_mov_b64 exec, 0xffff\n"
    "v_mov_b32 v40, v96\n"
    "v_mov_b32 v41, v100\n"
    "v_mov_b32 v42, v104\n"
    "v_mov_b32 v43, v108\n"
    "v_mov_b32 v44, v112\n"
    "v_mov_b32 v45, v116\n"
    "v_mov_b32 v46, v120\n"
    "v_mov_b32 v47, v124\n"
    "ds_write_b128 v12, v[40:43] offset:4096\n"
    "ds_write_b128 v12, v[44:47] offset:4112\n"
    "s_mov_b64 exec, -1\n"
    "v_mov_b32 v60, 0\n"
    "s_add_u32 s12, s11, 16384\n"
    "s_cmp_lt_u32 s3, 4\n"
    "s_waitcnt lgkmcnt(0)\n"
    "s_barrier\n"
    "ds_read_b32 v60, v60 offset:18944\n"
    "s_waitcnt lgkmcnt(0)\n"
    "v_add_u32 v11, v11, v60\n"
    "buffer_load_dwordx4 v[52:55], v11, s[36:39], s11 offen sc1\n"
    "buffer_load_dwordx4 v[56:59], v11, s[36:39], s12 offen sc1\n"
    "s_cbranch_scc0 .Lk_cchk_97\n"
    "ds_read2st64_b32 v[96:97], v22 offset0:16 offset1:17\n"
    "ds_read2st64_b32 v[98:99], v22 offset0:18 offset1:19\n"
    "ds_read2st64_b32 v[100:101], v22 offset0:20 offset1:21\n"
    "ds_read2st64_b32 v[102:103], v22 offset0:22 offset1:23\n"
    "ds_read2st64_b32 v[104:105], v22 offset0:24 offset1:25\n"
    "ds_read2st64_b32 v[106:107], v22 offset0:26 offset1:27\n"
    "ds_read2st64_b32 v[108:109], v22 offset0:28 offset1:29\n"
    "ds_read2st64_b32 v[110:111], v22 offset0:30 offset1:31\n"
    "s_waitcnt lgkmcnt(6)\n"
    "v_pk_add_f32 v[96:97], v[96:97], v[98:99]\n"
    "s_waitcnt lgkmcnt(4)\n"
    "v_pk_add_f32 v[100:101], v[100:101], v[102:103]\n"
    "s_waitcnt lgkmcnt(2)\n"
    "v_pk_add_f32 v[104:105], v[104:105], v[106:107]\n"
    "v_pk_add_f32 v[96:97], v[96:97], v[100:101]\n"
    "s_waitcnt lgkmcnt(0)\n"
    "v_pk_add_f32 v[108:109], v[108:109], v[110:111]\n"
    "v_pk_add_f32 v[104:105], v[104:105], v[108:109]\n"
    "v_pk_add_f32 v[96:97], v[96:97], v[104:105]\n"
    "v_add_f32 v26, v96, v97\n"
    "s_branch .Lk_cchk_97\n"
    ".Lk_cpoll_96:\n"
    "buffer_load_dwordx4 v[52:55], v11, s[36:39], s11 offen sc1\n"
    "buffer_load_dwordx4 v[56:59], v11, s[36:39], s12 offen sc1\n"
    ".Lk_cchk_97:\n"
    "s_waitcnt vmcnt(1)\n"
    "v_dot2_f32_bf16 v32, v52, v20, v48\n"
    "v_dot2_f32_bf16 v33, v52, v21, v49\n"
    "v_dot2_f32_bf16 v34, v54, v20, v50\n"
    "v_dot2_f32_bf16 v35, v54, v21, v51\n"
    "v_cmp_eq_u32_e64 s[54:55], s51, v53\n"
    "v_cmp_eq_u32_e64 s[58:59], s51, v55\n"
    "s_and_b64 s[54:55], s[54:55], s[58:59]\n"
    "s_waitcnt vmcnt(0)\n"
    "v_dot2_f32_bf16 v32, v56, v20, v32\n"
    "v_dot2_f32_bf16 v33, v56, v21, v33\n"
    "v_dot2_f32_bf16 v34, v58, v20, v34\n"
    "v_dot2_f32_bf16 v35, v58, v21, v35\n"
    "v_cmp_eq_u32_e64 s[56:57], s51, v57\n"
    "v_cmp_eq_u32_e64 s[58:59], s51, v59\n"
    "s_and_b64 s[56:57], s[56:57], s[58:59]\n"
    "s_and_b64 s[54:55], s[54:55], s[56:57]\n"
    "s_cmp_eq_u64 s[54:55], -1\n"
    "s_cbranch_scc1 .Lk_upc_16\n"
    "s_sub_u32 s52, s52, 1\n"
    "s_cmp_gt_i32 s52, 0\n"
    "s_cbranch_scc1 .Lk_cpoll_96\n"
    ".Lk_upc_16:\n"
    "v_add_f32_dpp v32, v32, v32 row_ror:4 row_mask:0xf bank_mask:0xf bound_ctrl:1\n"
    "v_add_f32_dpp v33, v33, v33 row_ror:4 row_mask:0xf bank_mask:0xf bound_ctrl:1\n"
    "v_add_f32_dpp v34, v34, v34 row_ror:4 row_mask:0xf bank_mask:0xf bound_ctrl:1\n"
    "v_add_f32_dpp v35, v35, v35 row_ror:4 row_mask:0xf bank_mask:0xf bound_ctrl:1\n"
    "v_add_f32_dpp v32, v32, v32 row_ror:8 row_mask:0xf bank_mask:0xf bound_ctrl:1\n"
    "v_add_f32_dpp v33, v33, v33 row_ror:8 row_mask:0xf bank_mask:0xf bound_ctrl:1\n"
    "v_add_f32_dpp v34, v34, v34 row_ror:8 row_mask:0xf bank_mask:0xf bound_ctrl:1\n"
    "v_add_f32_dpp v35, v35, v35 row_ror:8 row_mask:0xf bank_mask:0xf bound_ctrl:1\n"
    "v_cvt_pk_bf16_f32 v36, v32, v33\n"
    "v_cvt_pk_bf16_f32 v37, v34, v35\n"
    "s_cmp_lt_u32 s3, 4\n"
    "v_mov_b32_dpp v38, v36 quad_perm:[1,0,3,2] row_mask:0xf bank_mask:0xf bound_ctrl:1\n"
    "v_mov_b32_dpp v39, v37 quad_perm:[1,0,3,2] row_mask:0xf bank_mask:0xf bound_ctrl:1\n"
    "s_nop 1\n"
    "v_mfma_f32_16x16x32_bf16 v[96:99], v[36:39], v[64:67], 0\n"
    "v_mfma_f32_16x16x32_bf16 v[100:103], v[36:39], v[68:71], 0\n"
    "v_mfma_f32_16x16x32_bf16 v[104:107], v[36:39], v[72:75], 0\n"
    "v_mfma_f32_16x16x32_bf16 v[108:111], v[36:39], v[76:79], 0\n"
    "v_mfma_f32_16x16x32_bf16 v[112:115], v[36:39], v[80:83], 0\n"
    "v_mfma_f32_16x16x32_bf16 v[116:119], v[36:39], v[84:87], 0\n"
    "v_mfma_f32_16x16x32_bf16 v[120:123], v[36:39], v[88:91], 0\n"
    "v_mfma_f32_16x16x32_bf16 v[124:127], v[36:39], v[92:95], 0\n"
    "v_mfma_f32_16x16x32_bf16 v[52:55], v[36:39], v[16:19], 0\n"
    "v_mfma_f32_16x16x32_bf16 v[56:59], v[36:39], v[36:39], 0\n"
    "s_mov_b32 exec_hi, 0\n"
    "s_min_u32 exec_lo, s88, 0xffff\n"
    "s_cmp_lt_u32 s3, 4\n"
    "v_cndmask_b32_e64 v40, v96, v98, s[48:49]\n"
    "v_cndmask_b32_e64 v41, v100, v102, s[48:49]\n"
    "v_cndmask_b32_e64 v42, v104, v106, s[48:49]\n"
    "v_cndmask_b32_e64 v43, v108, v110, s[48:49]\n"
    "ds_write_b128 v12, v[40:43]\n"
    "v_cndmask_b32_e64 v44, v112, v114, s[48:49]\n"
    "v_cndmask_b32_e64 v45, v116, v118, s[48:49]\n"
    "v_cndmask_b32_e64 v46, v120, v122, s[48:49]\n"
    "v_cndmask_b32_e64 v47, v124, v126, s[48:49]\n"
    "ds_write_b128 v12, v[44:47] offset:16\n"
    "v_cndmask_b32_e64 v60, v52, v54, s[48:49]\n"
    "v_cndmask_b32_e64 v61, v56, v58, s[48:49]\n"
    "ds_write_b64 v14, v[60:61] offset:8192\n"
    "s_mov_b64 exec, s[88:89]\n"
    "s_waitcnt lgkmcnt(0)\n"
    "s_cbranch_execz .Lk_nb_98\n"
    "s_barrier\n"
    ".Lk_nb_98:\n"
    "s_cbranch_scc0 .Lk_end_1\n"
    "ds_read_b64 v[32:33], v23 offset:8192\n"
    "ds_read2st64_b32 v[96:97], v22 offset0:0 offset1:1\n"
    "ds_read2st64_b32 v[98:99], v22 offset0:2 offset1:3\n"
    "ds_read2st64_b32 v[100:101], v22 offset0:4 offset1:5\n"
    "ds_read2st64_b32 v[102:103], v22 offset0:6 offset1:7\n"
    "ds_read2st64_b32 v[104:105], v22 offset0:8 offset1:9\n"
    "ds_read2st64_b32 v[106:107], v22 offset0:10 offset1:11\n"
    "ds_read2st64_b32 v[108:109], v22 offset0:12 offset1:13\n"
    "ds_read2st64_b32 v[110:111], v22 offset0:14 offset1:15\n"
    "s_waitcnt lgkmcnt(8)\n"
    "v_add_f32_dpp v32, v32, v32 quad_perm:[1,0,3,2] row_mask:0xf bank_mask:0xf bound_ctrl:1\n"
    "v_add_f32_dpp v33, v33, v33 quad_perm:[1,0,3,2] row_mask:0xf bank_mask:0xf bound_ctrl:1\n"
    "s_waitcnt lgkmcnt(6)\n"
    "v_pk_add_f32 v[96:97], v[96:97], v[98:99]\n"
    "v_add_f32_dpp v32, v32, v32 quad_perm:[2,3,0,1] row_mask:0xf bank_mask:0xf bound_ctrl:1\n"
    "v_add_f32_dpp v33, v33, v33 quad_perm:[2,3,0,1] row_mask:0xf bank_mask:0xf bound_ctrl:1\n"
    "s_waitcnt lgkmcnt(4)\n"
    "v_pk_add_f32 v[100:101], v[100:101], v[102:103]\n"
    "v_add_f32_dpp v32, v32, v32 row_ror:4 row_mask:0xf bank_mask:0xf bound_ctrl:1\n"
    "v_add_f32_dpp v33, v33, v33 row_ror:4 row_mask:0xf bank_mask:0xf bound_ctrl:1\n"
    "s_waitcnt lgkmcnt(2)\n"
    "v_pk_add_f32 v[104:105], v[104:105], v[106:107]\n"
    "v_add_f32_dpp v32, v32, v32 row_ror:8 row_mask:0xf bank_mask:0xf bound_ctrl:1\n"
    "v_add_f32_dpp v33, v33, v33 row_ror:8 row_mask:0xf bank_mask:0xf bound_ctrl:1\n"
    "v_pk_add_f32 v[96:97], v[96:97], v[100:101]\n"
    "v_mul_f32 v32, 0x3b000000, v32\n"
    "v_mul_f32 v33, 0x3b000000, v33\n"
    "s_waitcnt lgkmcnt(0)\n"
    "v_pk_add_f32 v[108:109], v[108:109], v[110:111]\n"
    "v_fma_f32 v33, -v32, v32, v33\n"
    "v_pk_add_f32 v[104:105], v[104:105], v[108:109]\n"
    "v_max_f32 v33, 0, v33\n"
    "v_cvt_pk_bf16_f32 v35, v32, v33\n"
    "v_pk_add_f32 v[96:97], v[96:97], v[104:105]\n"
    "v_and_b32 v33, v9, v35\n"
    "v_lshlrev_b32 v32, 16, v35\n"
    "v_add_f32 v33, 0x3727c5ac, v33\n"
    "v_add_f32 v34, v96, v97\n"
    "v_cvt_pk_bf16_f32 v33, v33, v33\n"
    "v_fma_f32 v34, -v32, v26, v34\n"
    "v_and_b32 v33, v9, v33\n"
    "v_sqrt_f32 v33, v33\n"
    "s_nop 0\n"
    "v_cvt_pk_bf16_f32 v33, v33, v33\n"
    "v_and_b32 v33, v9, v33\n"
    "v_rcp_f32 v33, v33\n"
    "s_nop 0\n"
    "v_mul_f32 v34, v34, v33\n"
    "v_cvt_pk_bf16_f32 v34, v34, v34\n"
    "v_and_b32 v34, v9, v34\n"
    "v_mul_f32 v35, v34, v27\n"
    "v_exp_f32 v35, v35\n"
    "s_nop 0\n"
    "v_add_f32 v35, 1.0, v35\n"
    "v_rcp_f32 v35, v35\n"
    "s_nop 0\n"
    "v_mul_f32 v35, v34, v35\n"
    "v_cvt_pk_bf16_f32 v35, v35, v35\n"
    "v_and_b32 v35, v9, v35\n"
    "v_mul_f32_dpp v35, v34, v35 row_ror:8 row_mask:0xf bank_mask:0xf bound_ctrl:1\n"
    "s_nop 1\n"
    "v_mov_b32_dpp v32, v35 quad_perm:[1,0,3,2] row_mask:0xf bank_mask:0xf bound_ctrl:1\n"
    "v_cvt_pk_bf16_f32 v32, v35, v32\n"
    "s_nop 1\n"
    "v_mov_b32_dpp v36, v32 row_newbcast:0 row_mask:0xf bank_mask:0xf bound_ctrl:1\n"
    "v_mov_b32_dpp v37, v32 row_newbcast:2 row_mask:0xf bank_mask:0xf bound_ctrl:1\n"
    "v_mov_b32_dpp v38, v32 row_newbcast:4 row_mask:0xf bank_mask:0xf bound_ctrl:1\n"
    "v_mov_b32_dpp v39, v32 row_newbcast:6 row_mask:0xf bank_mask:0xf bound_ctrl:1\n"
    "s_nop 1\n"
    "v_mfma_f32_16x16x32_bf16 v[96:99], v[36:39], v[128:131], 0\n"
    "v_mfma_f32_16x16x32_bf16 v[100:103], v[36:39], v[132:135], 0\n"
    "v_mfma_f32_16x16x32_bf16 v[104:107], v[36:39], v[136:139], 0\n"
    "v_mfma_f32_16x16x32_bf16 v[108:111], v[36:39], v[140:143], 0\n"
    "v_mfma_f32_16x16x32_bf16 v[112:115], v[36:39], v[144:147], 0\n"
    "v_mfma_f32_16x16x32_bf16 v[116:119], v[36:39], v[148:151], 0\n"
    "v_mfma_f32_16x16x32_bf16 v[120:123], v[36:39], v[152:155], 0\n"
    "v_mfma_f32_16x16x32_bf16 v[124:127], v[36:39], v[156:159], 0\n"
    "s_cbranch_execz .Lk_end_1\n"
    "s_nop 0\n"
    "v_cvt_pk_bf16_f32 v28, v96, v100\n"
    "s_nop 0\n"
    "v_cvt_pk_bf16_f32 v30, v104, v108\n"
    "s_mov_b32 exec_lo, 0\n"
    "v_cvt_pk_bf16_f32 v28, v112, v116\n"
    "s_nop 0\n"
    "v_cvt_pk_bf16_f32 v30, v120, v124\n"
    "s_mov_b32 exec_lo, -1\n"
    "buffer_store_dwordx4 v[28:31], v15, s[36:39], s13 offen sc1\n"
    ".Lk_end_1:\n"
    "s_cbranch_execnz .Lk_real_99\n"
    "s_setpc_b64 s[90:91]\n"
    ".Lk_real_99:\n"
    "s_endpgm\n"
    ".Lk_code_end:\n"
    :
    : "{v0}"(threadIdx.x), "{s2}"(blockIdx.x), "{s[0:1]}"(__builtin_amdgcn_kernarg_segment_ptr()), "s"((__attribute__((address_space(3))) float*)lds)
    : "v1", "v2", "v3", "v4", "v5", "v6", "v7", "v8", "v9", "v10", "v11", "v12", "v13", "v14", "v15", "v16", "v17", "v18", "v19", "v20", "v21", "v22", "v23", "v24", "v25", "v26", "v27", "v28", "v29", "v30", "v31", "v32", "v33", "v34", "v35", "v36", "v37", "v38", "v39", "v40", "v41", "v42", "v43", "v44", "v45", "v46", "v47", "v48", "v49", "v50", "v51", "v52", "v53", "v54", "v55", "v56", "v57", "v58", "v59", "v60", "v61", "v62", "v63", "v64", "v65", "v66", "v67", "v68", "v69", "v70", "v71", "v72", "v73", "v74", "v75", "v76", "v77", "v78", "v79", "v80", "v81", "v82", "v83", "v84", "v85", "v86", "v87", "v88", "v89", "v90", "v91", "v92", "v93", "v94", "v95", "v96", "v97", "v98", "v99", "v100", "v101", "v102", "v103", "v104", "v105", "v106", "v107", "v108", "v109", "v110", "v111", "v112", "v113", "v114", "v115", "v116", "v117", "v118", "v119", "v120", "v121", "v122", "v123", "v124", "v125", "v126", "v127", "v128", "v129", "v130", "v131", "v132", "v133", "v134", "v135", "v136", "v137", "v138", "v139", "v140", "v141", "v142", "v143", "v144", "v145", "v146", "v147", "v148", "v149", "v150", "v151", "v152", "v153", "v154", "v155", "v156", "v157", "v158", "v159", "v160", "v161", "v162", "v163", "s4", "s5", "s6", "s7", "s8", "s9", "s10", "s11", "s12", "s13", "s14", "s15", "s16", "s17", "s18", "s19", "s20", "s21", "s22", "s23", "s24", "s25", "s26", "s27", "s28", "s29", "s30", "s31", "s33", "s34", "s35", "s36", "s37", "s38", "s39", "s40", "s41", "s42", "s43", "s44", "s45", "s46", "s47", "s48", "s49", "s50", "s51", "s52", "s53", "s54", "s55", "s56", "s57", "s58", "s59", "s60", "s61", "s62", "s63", "s64", "s65", "s66", "s67", "s68", "s69", "s70", "s71", "s72", "s73", "s74", "s75", "s76", "s77", "s78", "s79", "s80", "s81", "s82", "s83", "s84", "s85", "s86", "s87", "s88", "s89", "s90", "s91", "s92", "s93", "s94", "s95", "s96", "s97", "s98", "s99", "vcc", "scc", "memory");
#endif
}

extern "C" void kernel_launch(void* const* d_in, const int* in_sizes, int n_in, void* d_out, int out_size, void* d_ws, size_t ws_size,
                              hipStream_t stream) {
  const size_t win = (size_t)(65536 - 4096) * 128;
  const uint32_t kvb = (uint32_t)(((size_t)8 * 65536 * 128 - win) * 2);
  Args a{{(const bf16_t*)d_in[2] + win, kvb, 0x20000u}, {(const bf16_t*)d_in[1] + win, kvb, 0x20000u}, {(const bf16_t*)d_in[3], 512u * 4096u * 2u, 0x20000u},
         (const bf16_t*)d_in[0], (unsigned char*)d_ws + 0, (const bf16_t*)d_in[4], (const bf16_t*)d_in[6], (const bf16_t*)d_in[7],
         (const bf16_t*)d_in[8], (bf16_t*)d_out};
  fused_decode<<<256, NTHR, 0, stream>>>(a);
}
```
